```python
import torch

def _softmax(x, axis=-1):
    return torch.softmax(x, dim=axis)
def _gelu(x):
    return torch.nn.functional.gelu(x, approximate="tanh")
def _silu(x):
    return torch.nn.functional.silu(x)
def _sigmoid(x):
    return torch.sigmoid(x)
def _layernorm(x, axis=-1, eps=1e-5):
    m = x.mean(dim=axis, keepdim=True)
    v = x.var(dim=axis, keepdim=True, unbiased=False)
    return (x - m) / torch.sqrt(v + eps)
def _scatter_add(tgt, idx, src, axis=0):
    return tgt.clone().index_add_(axis, idx, src)
def _segment_sum(src, seg, n):
    out = torch.zeros((n,) + src.shape[1:], dtype=src.dtype)
    return out.index_add_(0, seg, src)
def _pad(x, pw):
    flat = tuple(v for pair in reversed(pw) for v in pair)
    return torch.nn.functional.pad(x, flat)

kernel_name = 'parallel_sum__linear_attn_block__mixer_token_mlp__prompt'

def setup_inputs():

    _w = (lambda a: (lambda t: t.to(torch.bfloat16) if t.is_floating_point() else t)(torch.as_tensor(a)))
    def _A_setup():
        B, S, D = 1, 2048, 640
        return {
            "x": _w((lambda *s: torch.randn(*s, dtype=torch.bfloat16) * 0.05)(B, S, D)),
            "wqkv": _w((lambda *s: torch.randn(*s, dtype=torch.bfloat16) * 0.05)(D, 3 * D)),
            "wo": _w((lambda *s: torch.randn(*s, dtype=torch.bfloat16) * 0.05)(D, D)),
        }
    _A_in = _A_setup()

    def _B_setup():
        B, S, D, F = 1, 2048, 640, 1728
        return {
            "x": _w((lambda *s: torch.randn(*s, dtype=torch.bfloat16) * 0.05)(B, S, D)),
            "w1": _w((lambda *s: torch.randn(*s, dtype=torch.bfloat16) * 0.4)(S, F) * (S ** -0.5)),
            "b1": _w((lambda *s: torch.randn(*s, dtype=torch.bfloat16) * 3.2)(F) * 0.02),
            "w2": _w((lambda *s: torch.randn(*s, dtype=torch.bfloat16) * 0.2)(F, S) * (F ** -0.5)),
            "b2": _w((lambda *s: torch.randn(*s, dtype=torch.bfloat16) * 0.4)(S) * 0.02),
        }
    _B_in = _B_setup()

    return {
        "A_x": _A_in["x"],
        "A_wqkv": _A_in["wqkv"],
        "A_wo": _A_in["wo"],
        "B_x": _B_in["x"],
        "B_w1": _B_in["w1"],
        "B_b1": _B_in["b1"],
        "B_w2": _B_in["w2"],
        "B_b2": _B_in["b2"],
    }

def reference(A_x, A_wqkv, A_wo, B_x, B_w1, B_b1, B_w2, B_b2):
    def _A_ref(x, wqkv, wo):
        B, S, D = x.shape
        H = 32; Dh = D // H
        qkv = (lambda x, *d: x.permute(*d))((x @ wqkv).reshape(B, S, 3, H, Dh), 2, 0, 3, 1, 4)
        q, k, v = qkv[0], qkv[1], qkv[2]
        phi = lambda t: torch.where(t > 0, t + 1, torch.exp(t)) + 1e-6
        pq, pk = phi(q), phi(k)
        kv = torch.einsum("bhsd,bhse->bhde", pk, v)
        z = torch.einsum("bhsd->bhd", pk)
        num = torch.einsum("bhqd,bhde->bhqe", pq, kv)
        den = torch.einsum("bhqd,bhd->bhq", pq, z)[..., None] + 1e-6
        o = num / den
        return (lambda x, *d: x.permute(*d))(o, 0, 2, 1, 3).reshape(B, S, D) @ wo

    def _B_ref(x, w1, b1, w2, b2):
        B, S, D = x.shape
        h = _layernorm(x, axis=-1)
        u = torch.einsum("bsd,sf->bdf", h, w1) + b1
        u = _gelu(u)
        y = torch.einsum("bdf,fs->bds", u, w2) + b2
        return (lambda x, *d: x.permute(*d))(y, 0, 2, 1)

    x = A_x
    xn = _layernorm(x, axis=-1)
    return x + _A_ref(xn, A_wqkv, A_wo) + _B_ref(xn, B_w1, B_b1, B_w2, B_b2)
```

```cpp
#include <hip/hip_runtime.h>
#include <hip/hip_bf16.h>
typedef __hip_bfloat16 bf16;
constexpr int S=2048, D=640, F=1728, H=32, DH=20;
__device__ __forceinline__ float b2f(bf16 v){return __bfloat162float(v);}
__device__ __forceinline__ bf16 f2b(float v){return __float2bfloat16(v);}
__device__ __forceinline__ float rb(float v){return b2f(f2b(v));}

__global__ void ln_k(const bf16* x, bf16* xn, bf16* h){
  int s=blockIdx.x; int t=threadIdx.x;
  float v[10]; float sum=0;
  for(int i=0;i<10;i++){v[i]=b2f(x[s*D+t+64*i]); sum+=v[i];}
  for(int o=32;o>0;o>>=1) sum+=__shfl_xor(sum,o);
  float m=sum/D; float q=0;
  for(int i=0;i<10;i++){float d=v[i]-m; q+=d*d;}
  for(int o=32;o>0;o>>=1) q+=__shfl_xor(q,o);
  float r=1.0f/sqrtf(q/D+1e-5f);
  sum=0;
  for(int i=0;i<10;i++){v[i]=rb((v[i]-m)*r); xn[s*D+t+64*i]=f2b(v[i]); sum+=v[i];}
  for(int o=32;o>0;o>>=1) sum+=__shfl_xor(sum,o);
  m=sum/D; q=0;
  for(int i=0;i<10;i++){float d=v[i]-m; q+=d*d;}
  for(int o=32;o>0;o>>=1) q+=__shfl_xor(q,o);
  r=1.0f/sqrtf(q/D+1e-5f);
  for(int i=0;i<10;i++){h[s*D+t+64*i]=f2b((v[i]-m)*r);}
}
__global__ void qkv_k(const bf16* xn,const bf16* w,float* qkv){
  int n=blockIdx.x*256+threadIdx.x; int s=blockIdx.y; if(n>=3*D) return;
  float a=0; for(int k=0;k<D;k++) a+=b2f(xn[s*D+k])*b2f(w[k*3*D+n]);
  a=rb(a);
  if(n<2*D){ a = (a>0? a+1.f: expf(a))+1e-6f; a=rb(a);}
  qkv[s*3*D+n]=a;
}
__global__ void kv_k(const float* qkv,float* kv,float* z){
  int h=blockIdx.x; int t=threadIdx.x; if(t>=420) return;
  if(t<400){int d=t/20,e=t%20; float a=0; for(int s=0;s<S;s++) a+=qkv[s*3*D+D+h*20+d]*qkv[s*3*D+2*D+h*20+e]; kv[h*400+t]=a;}
  else {int d=t-400; float a=0; for(int s=0;s<S;s++) a+=qkv[s*3*D+D+h*20+d]; z[h*20+d]=a;}
}
__global__ void o_k(const float* qkv,const float* kv,const float* z,bf16* o){
  int s=blockIdx.x; int j=threadIdx.x; int h=j/20,e=j%20;
  float num=0,den=0; for(int d=0;d<20;d++){float p=qkv[s*3*D+h*20+d]; num+=p*kv[h*400+d*20+e]; den+=p*z[h*20+d];}
  o[s*D+j]=f2b(num/(den+1e-6f));
}
__global__ void u_k(const bf16* h,const bf16* w1,const bf16* b1,bf16* g){
  int f=blockIdx.x*64+threadIdx.x; int d=blockIdx.y;
  float a=0; for(int s=0;s<S;s++) a+=b2f(h[s*D+d])*b2f(w1[s*F+f]);
  a+=b2f(b1[f]);
  float t=tanhf(0.7978845608f*(a+0.044715f*a*a*a));
  g[f*D+d]=f2b(0.5f*a*(1.f+t));
}
__global__ void out_k(const bf16* x,const bf16* o,const bf16* wo,const bf16* g,const bf16* w2,const bf16* b2,bf16* out){
  int d=threadIdx.x; int s=blockIdx.x;
  float a=0; for(int j=0;j<D;j++) a+=b2f(o[s*D+j])*b2f(wo[j*D+d]);
  float y=0; for(int f=0;f<F;f++) y+=b2f(g[f*D+d])*b2f(w2[f*S+s]);
  y+=b2f(b2[s]);
  out[s*D+d]=f2b(b2f(x[s*D+d])+a+y);
}
extern "C" void kernel_launch(void* const* d_in, const int* in_sizes, int n_in,
                              void* d_out, int out_size, void* d_ws, size_t ws_size,
                              hipStream_t stream) {
  const bf16* x=(const bf16*)d_in[0]; const bf16* wqkv=(const bf16*)d_in[1]; const bf16* wo=(const bf16*)d_in[2];
  const bf16* w1=(const bf16*)d_in[4]; const bf16* b1=(const bf16*)d_in[5]; const bf16* w2=(const bf16*)d_in[6]; const bf16* b2=(const bf16*)d_in[7];
  char* ws=(char*)d_ws;
  bf16* xn=(bf16*)ws; bf16* h=(bf16*)(ws+(4<<20)); bf16* o=(bf16*)(ws+(8<<20)); bf16* g=(bf16*)(ws+(12<<20));
  float* kv=(float*)(ws+(16<<20)); float* z=(float*)(ws+(17<<20)); float* qkv=(float*)(ws+(32<<20));
  ln_k<<<S,64,0,stream>>>(x,xn,h);
  qkv_k<<<dim3(8,S),256,0,stream>>>(xn,wqkv,qkv);
  kv_k<<<H,448,0,stream>>>(qkv,kv,z);
  o_k<<<S,640,0,stream>>>(qkv,kv,z,o);
  u_k<<<dim3(F/64,D),64,0,stream>>>(h,w1,b1,g);
  out_k<<<S,640,0,stream>>>(x,o,wo,g,w2,b2,(bf16*)d_out);
}
```

```cpp
#include <hip/hip_runtime.h>
#include <hip/hip_bf16.h>

typedef short s16x4 __attribute__((ext_vector_type(4)));
typedef short s16x8 __attribute__((ext_vector_type(8)));
typedef __bf16 bf16x8 __attribute__((ext_vector_type(8)));
typedef __bf16 bf16x2 __attribute__((ext_vector_type(2)));
typedef float f32x2 __attribute__((ext_vector_type(2)));
typedef float f32x4 __attribute__((ext_vector_type(4)));
typedef float f32x16 __attribute__((ext_vector_type(16)));
typedef unsigned u32x2 __attribute__((ext_vector_type(2)));
typedef unsigned u32x4 __attribute__((ext_vector_type(4)));
typedef __amdgpu_buffer_rsrc_t rsrc_t;
#define LAS __attribute__((address_space(3)))
#define DI __device__ __forceinline__

constexpr int S = 2048, D = 640, F = 1728, N3 = 1920;
constexpr unsigned HT_OFF = 0;
constexpr unsigned AP_OFF = HT_OFF + 256 * 10240;
constexpr unsigned OT_OFF = AP_OFF + 296 * 10240;
constexpr unsigned QT_OFF = OT_OFF + 640 * 4096;
constexpr unsigned ST_OFF = QT_OFF + 256 * 30720;
constexpr unsigned CS_OFF = ST_OFF + 2 * 2048 * 4;
constexpr unsigned FL_OFF = CS_OFF + 2048 * 4;
constexpr unsigned WS_END = FL_OFF + 8192;
constexpr unsigned OOB = 0x7fff0000u;
constexpr int SC1 = 16;
constexpr int LDS_BYTES = 160 * 1024;

struct Params {
  const void *x, *wqkv, *wo, *w1, *b1, *w2, *b2;
  void *out, *ws;
};

DI rsrc_t mk(const void* p, unsigned bytes) { return __builtin_amdgcn_make_buffer_rsrc((void*)p, (short)0, (int)bytes, 0x00020000); }
DI float bflo(unsigned u) { return __builtin_bit_cast(float, u << 16); }
DI float bfhi(unsigned u) { return __builtin_bit_cast(float, u & 0xffff0000u); }
DI unsigned pk2(float a, float b) { bf16x2 v = {(__bf16)a, (__bf16)b}; return __builtin_bit_cast(unsigned, v); }
template <int C> DI float dpp(float v) { return __builtin_bit_cast(float, __builtin_amdgcn_update_dpp(0, __builtin_bit_cast(int, v), C, 0xf, 0xf, true)); }
DI float wave_sum(float v) {
  v += dpp<0xB1>(v);
  v += dpp<0x4E>(v);
  v += dpp<0x141>(v);
  v += dpp<0x140>(v);
  unsigned u = __builtin_bit_cast(unsigned, v);
  auto a = __builtin_amdgcn_permlane16_swap(u, u, false, false);
  v = __builtin_bit_cast(float, (unsigned)a[0]) + __builtin_bit_cast(float, (unsigned)a[1]);
  u = __builtin_bit_cast(unsigned, v);
  auto b = __builtin_amdgcn_permlane32_swap(u, u, false, false);
  return __builtin_bit_cast(float, (unsigned)b[0]) + __builtin_bit_cast(float, (unsigned)b[1]);
}
DI s16x4 trd(unsigned a) { return __builtin_amdgcn_ds_read_tr16_b64_v4i16((LAS s16x4*)a); }
DI bf16x8 trd2(unsigned a, unsigned d) { s16x4 lo = trd(a), hi = trd(a + d); return __builtin_bit_cast(bf16x8, __builtin_shufflevector(lo, hi, 0, 1, 2, 3, 4, 5, 6, 7)); }
DI bf16x8 rd128(unsigned a) { return *(LAS bf16x8*)a; }
DI void bar() { asm volatile("" ::: "memory");
 __builtin_amdgcn_s_barrier();
 asm volatile("" ::: "memory"); }
DI void bar_l() { asm volatile("s_waitcnt lgkmcnt(0)" ::: "memory"); __builtin_amdgcn_s_barrier(); asm volatile("" ::: "memory"); }
template <int N> DI void wait_v() { asm volatile("s_waitcnt vmcnt(%0)" ::"i"(N) : "memory"); }
DI void dma(unsigned voff, rsrc_t r, unsigned soff, unsigned lds) {
  asm volatile("s_mov_b32 m0, %3\n\ts_nop 0\n\tbuffer_load_dwordx4 %0, %1, %2 offen lds" ::"v"(voff), "s"(r), "s"(soff), "s"(lds) : "memory");
}
DI void dma_sc1(unsigned voff, rsrc_t r, unsigned soff, unsigned lds) {
  asm volatile("s_mov_b32 m0, %3\n\ts_nop 0\n\tbuffer_load_dwordx4 %0, %1, %2 offen sc1 lds" ::"v"(voff), "s"(r), "s"(soff), "s"(lds) : "memory");
}

#define STAMP(i)

DI void phase_ln(const Params& p, int w, int l) {
  const rsrc_t rx = mk(p.x, S * D * 2), rw = mk(p.ws, WS_END);
  const int bid = blockIdx.x, row = 8 * bid + w;
  constexpr unsigned LB = 128 * 1024, RS = 1344;
  unsigned u[5];
#pragma unroll
  for (int j = 0; j < 5; ++j) u[j] = __builtin_amdgcn_raw_buffer_load_b32(rx, l * 4 + j * 256, row * 1280, 0);
  float v[10];
  float s = 0.f;
#pragma unroll
  for (int j = 0; j < 5; ++j) { v[2 * j] = bflo(u[j]); v[2 * j + 1] = bfhi(u[j]); s += v[2 * j] + v[2 * j + 1]; }
  const float m = wave_sum(s) * (1.f / D);
  float q = 0.f;
#pragma unroll
  for (int j = 0; j < 10; ++j) { v[j] -= m; q += v[j] * v[j]; }
  const float r = __builtin_amdgcn_rsqf(wave_sum(q) * (1.f / D) + 1e-5f);
  float s2 = 0.f, q2 = 0.f;
#pragma unroll
  for (int j = 0; j < 5; ++j) {
    const unsigned t = pk2(v[2 * j] * r, v[2 * j + 1] * r);
    v[2 * j] = bflo(t); v[2 * j + 1] = bfhi(t);
    s2 += v[2 * j] + v[2 * j + 1]; q2 += v[2 * j] * v[2 * j] + v[2 * j + 1] * v[2 * j + 1];
  }
  const float m2 = wave_sum(s2) * (1.f / D);
  const float var2 = fmaxf(wave_sum(q2) * (1.f / D) - m2 * m2, 0.f);
  const float r2 = __builtin_amdgcn_rsqf(var2 + 1e-5f);
#pragma unroll
  for (int j = 0; j < 5; ++j) *(LAS unsigned*)(LB + w * RS + l * 4 + j * 256) = pk2((v[2 * j] - m2) * r2, (v[2 * j + 1] - m2) * r2);
  {
    const float sv = (l & 1) ? -m * r : r;
    __builtin_amdgcn_raw_buffer_store_b32(__builtin_bit_cast(unsigned, sv), rw, l < 2 ? ST_OFF + l * 8192 + row * 4 : OOB, 0, SC1);
  }
  bar_l();
  const unsigned ta = LB + ((l & 15) >> 2) * RS + (l >> 4) * 32 + (l & 3) * 8;
  {
    bf16x8 c = trd2(ta + w * 128, 4 * RS);
    __builtin_amdgcn_raw_buffer_store_b128(__builtin_bit_cast(u32x4, c), rw, HT_OFF + l * 16, bid * 10240 + w * 1024, SC1);
  }
  if (w < 2) {
    bf16x8 c = trd2(ta + (w + 8) * 128, 4 * RS);
    __builtin_amdgcn_raw_buffer_store_b128(__builtin_bit_cast(u32x4, c), rw, HT_OFF + l * 16, bid * 10240 + (w + 8) * 1024, SC1);
  }
}

DI void phase_aux(const Params& p, int a, int w, int l) {
  const rsrc_t rw = mk(p.ws, WS_END), rwo = mk(p.wo, D * D * 2), rq = mk(p.wqkv, D * N3 * 2);
  for (int j = w; j < 50; j += 8) dma(l * 16, rwo, a * 51200 + j * 1024, j * 1024);
  const int t = threadIdx.x;
  float acc[8] = {0, 0, 0, 0, 0, 0, 0, 0};
  if (a < 15) {
    u32x4 v[20];
#pragma unroll
    for (int j = 0; j < 20; ++j) v[j] = __builtin_amdgcn_raw_buffer_load_b128(rq, (t >> 4) * 3840 + (t & 15) * 16, a * 256 + j * 32 * 3840, 0);
#pragma unroll
    for (int j = 0; j < 20; ++j)
#pragma unroll
      for (int e = 0; e < 4; ++e) { acc[2 * e] += bflo(v[j][e]); acc[2 * e + 1] += bfhi(v[j][e]); }
  }
  constexpr unsigned CB = 64 * 1024;
  *(LAS f32x4*)(CB + t * 32) = f32x4{acc[0], acc[1], acc[2], acc[3]};
  *(LAS f32x4*)(CB + t * 32 + 16) = f32x4{acc[4], acc[5], acc[6], acc[7]};
  wait_v<0>();
  bar_l();
  if (t < 128 && a < 15) {
    float s = 0.f;
#pragma unroll
    for (int j = 0; j < 32; ++j) s += *(LAS float*)(CB + j * 512 + t * 4);
    __builtin_amdgcn_raw_buffer_store_b32(__builtin_bit_cast(unsigned, s), rw, CS_OFF + (a * 128 + t) * 4, 0, SC1);
  }
  const unsigned ta = ((l & 15) >> 2) * 1280 + (l >> 4) * 32 + (l & 3) * 8;
  for (int j = w; j < 50; j += 8) {
    const int c = j / 10, cb = j % 10;
    bf16x8 v = trd2(ta + c * 10240 + cb * 128, 4 * 1280);
    __builtin_amdgcn_raw_buffer_store_b128(__builtin_bit_cast(u32x4, v), rw, AP_OFF + l * 16, (216 + 5 * a + c) * 10240 + cb * 1024, SC1);
  }
}

DI void phase_qkv(const Params& p, int rp, int ct, int w, int l) {
  const rsrc_t rx = mk(p.x, S * D * 2), rq = mk(p.wqkv, D * N3 * 2), rw = mk(p.ws, WS_END);
  const int g = l >> 4, i16 = l & 15;
  const int row0 = rp * 128, col0 = ct * 128;
  constexpr unsigned SLOT = 32768;
  unsigned vx[2], vw[2];
#pragma unroll
  for (int u = 0; u < 2; ++u) {
    const int j = 2 * w + u;
    vx[u] = (row0 + 8 * j + (l >> 3)) * 1280 + (((l & 7) ^ ((j & 3) * 2 + ((l >> 4) & 1))) * 16);
    const int f = g | ((w & 1) << 2);
    vw[u] = (4 * j + g) * 3840 + col0 * 2 + (((((i16 >> 1) ^ f) << 1) | (i16 & 1)) * 16);
  }
  auto issue = [&](int k) {
    const unsigned sb = (k & 3) * SLOT + w * 2048;
    dma(vx[0], rx, k * 128, sb);
    dma(vx[1], rx, k * 128, sb + 1024);
    dma(vw[0], rq, k * (64 * 3840), sb + 16384);
    dma(vw[1], rq, k * (64 * 3840), sb + 16384 + 1024);
  };
  issue(0); issue(1); issue(2);
  const int mh = w >> 2, nq = w & 3;
  unsigned ra[2], rb[2];
#pragma unroll
  for (int ks = 0; ks < 2; ++ks) ra[ks] = (64 * mh + 8 * (i16 >> 2) + (i16 & 3)) * 128 + (((4 * ks + g) ^ (i16 >> 1)) * 16);
#pragma unroll
  for (int n = 0; n < 2; ++n) {
    const int q = i16 >> 2, pp = l & 3, f = q | ((g & 1) << 2);
    rb[n] = 16384 + (8 * g + q) * 256 + (((2 * nq + n) ^ f) * 32) + 8 * pp;
  }
  f32x4 acc[4][2];
#pragma unroll
  for (int m = 0; m < 4; ++m) { acc[m][0] = f32x4{0, 0, 0, 0}; acc[m][1] = f32x4{0, 0, 0, 0}; }
#pragma unroll 1
  for (int k = 0; k < 10; ++k) {
    if (k < 8) wait_v<8>(); else if (k == 8) wait_v<4>(); else wait_v<0>();
    bar();
    if (k + 3 < 10) issue(k + 3);
    const unsigned sb = (k & 3) * SLOT;
#pragma unroll
    for (int ks = 0; ks < 2; ++ks) {
      bf16x8 a[4], b[2];
#pragma unroll
      for (int n = 0; n < 2; ++n) b[n] = trd2(sb + rb[n] + ks * 8192, 1024);
#pragma unroll
      for (int m = 0; m < 4; ++m) a[m] = rd128(sb + ra[ks] + (m >> 1) * 4096 + (m & 1) * 512);
#pragma unroll
      for (int m = 0; m < 4; ++m)
#pragma unroll
        for (int n = 0; n < 2; ++n) acc[m][n] = __builtin_amdgcn_mfma_f32_16x16x32_bf16(a[m], b[n], acc[m][n], 0, 0, 0);
    }
  }
  STAMP(3);
  float cs[2];
#pragma unroll
  for (int n = 0; n < 2; ++n) cs[n] = __builtin_bit_cast(float, __builtin_amdgcn_raw_buffer_load_b32(rw, CS_OFF + (col0 + 32 * nq + 16 * n + i16) * 4, 0, SC1));
  f32x4 rr[4], mr[4];
#pragma unroll
  for (int m = 0; m < 4; ++m) {
    const unsigned o = ST_OFF + (row0 + 64 * mh + 32 * (m >> 1) + 8 * g + 4 * (m & 1)) * 4;
    rr[m] = __builtin_bit_cast(f32x4, __builtin_amdgcn_raw_buffer_load_b128(rw, o, 0, SC1));
    mr[m] = __builtin_bit_cast(f32x4, __builtin_amdgcn_raw_buffer_load_b128(rw, o + 8192, 0, SC1));
  }
#pragma unroll
  for (int m = 0; m < 4; ++m)
#pragma unroll
    for (int n = 0; n < 2; ++n) acc[m][n] = acc[m][n] * rr[m] + mr[m] * cs[n];
  if (ct < 10) {
#pragma unroll
    for (int m = 0; m < 4; ++m)
#pragma unroll
      for (int n = 0; n < 2; ++n)
#pragma unroll
        for (int r = 0; r < 4; ++r) {
          const float t = acc[m][n][r];
          acc[m][n][r] = (fmaxf(t, 0.f) + 1e-6f) + __builtin_amdgcn_exp2f(fminf(t * 1.44269504088896f, 0.f));
        }
  }
#pragma unroll
  for (int P = 0; P < 2; ++P)
#pragma unroll
    for (int n = 0; n < 2; ++n) {
      const f32x4 lo = acc[2 * P][n], hi = acc[2 * P + 1][n];
      u32x4 o = {pk2(lo[0], lo[1]), pk2(lo[2], lo[3]), pk2(hi[0], hi[1]), pk2(hi[2], hi[3])};
      const unsigned chunk = (row0 + 64 * mh + 32 * P) / 8 + g;
      __builtin_amdgcn_raw_buffer_store_b128(o, rw, QT_OFF + chunk * 30720 + (col0 + 32 * nq + 16 * n + i16) * 16, 0, SC1);
    }
}

#define SCA "sc1"
#define SCB "sc1"
struct Frag { bf16x8 a[5]; bf16x8 b; };
constexpr unsigned GSLOT = 18432;
constexpr int GD = 8;
DI void g_issue(bool real, unsigned va, rsrc_t ra, unsigned sa, unsigned vb, rsrc_t rb, unsigned sb, unsigned ldsA, unsigned dB) {
  unsigned long long m1 = real ? ~0ull : 1ull, m2 = real ? 0xffffull : 1ull, m3 = m1;
  asm volatile(
      "s_mov_b64 exec, %8\n\ts_mov_b32 m0, %6\n\ts_nop 0\n\t"
      "buffer_load_dwordx4 %0, %1, %2 offen " SCA " lds\n\t"
      "s_mov_b64 exec, %9\n\t"
      "buffer_load_dwordx4 %0, %1, %2 offen offset:1024 " SCA " lds\n\t"
      "s_mov_b64 exec, %10\n\ts_add_u32 m0, %6, %7\n\ts_nop 0\n\t"
      "buffer_load_dwordx4 %3, %4, %5 offen " SCB " lds\n\t"
      "s_mov_b64 exec, -1"
      ::"v"(va), "s"(ra), "s"(sa), "v"(vb), "s"(rb), "s"(sb), "s"(ldsA), "s"(dB), "s"(m1), "s"(m2), "s"(m3) : "memory", "scc");
}
template <int MODE>
DI void gemm6480(const Params& p, f32x4 (&acc)[5], int dp, int col0, int w, int l) {
  constexpr int NST = MODE == 0 ? 32 : 37;
  constexpr unsigned RSB = MODE == 0 ? 3456 : 4096;
  const rsrc_t rw = mk(p.ws, WS_END);
  const rsrc_t rb0 = MODE == 0 ? mk(p.w1, S * F * 2) : mk(p.w2, S * F * 2);
  const int g = l >> 4, i16 = l & 15;
  const unsigned va = (MODE == 0 ? HT_OFF : AP_OFF) + w * 10240 + dp * 1280 + l * 16;
  const int fb = ((l >> 4) & 1) | ((w & 1) << 1);
  const unsigned vb = (8 * w + (l >> 3)) * RSB + col0 * 2 + ((((((l & 7) >> 1) ^ fb) << 1) | (l & 1)) * 16);
  const unsigned dB = 10240 + w * 1024 - w * 1280;
  auto issue = [&](int k) {
    const unsigned ldsA = (k & (GD - 1)) * GSLOT + w * 1280;
    if (MODE == 1 && k >= 27) g_issue(k < NST, va, rw, k * 81920, vb, rw, OT_OFF + (k - 27) * (64 * RSB), ldsA, dB);
    else g_issue(k < NST, va, rw, k * 81920, vb, rb0, k * (64 * RSB), ldsA, dB);
  };
#pragma unroll 1
  for (int k = 0; k < GD - 1; ++k) issue(k);
  const int kh = w >> 2, nq = w & 3;
  const unsigned la = (4 * kh + g) * 1280 + i16 * 16;
  const int q = i16 >> 2;
  const unsigned lb = 10240 + (32 * kh + 8 * g + q) * 128 + ((nq ^ (((q >> 1) & 1) | ((g & 1) << 1))) * 32) + 8 * (l & 3);
  auto rd = [&](Frag& f, int k) {
    const unsigned sb = (k & (GD - 1)) * GSLOT;
    f.b = trd2(sb + lb, 512);
#pragma unroll
    for (int j = 0; j < 5; ++j) f.a[j] = rd128(sb + la + j * 256);
  };
  auto mm = [&](const Frag& f) {
#pragma unroll
    for (int j = 0; j < 5; ++j)
      acc[j] = MODE == 0 ? __builtin_amdgcn_mfma_f32_16x16x32_bf16(f.b, f.a[j], acc[j], 0, 0, 0)
                         : __builtin_amdgcn_mfma_f32_16x16x32_bf16(f.a[j], f.b, acc[j], 0, 0, 0);
  };
#pragma unroll
  for (int j = 0; j < 5; ++j) acc[j] = f32x4{0, 0, 0, 0};
  Frag f0, f1;
  wait_v<3 * (GD - 2)>();
  bar();
  rd(f0, 0);
#pragma unroll 1
  for (int k = 0; k + 1 < NST; k += 2) {
    wait_v<3 * (GD - 3)>();
    bar();
    issue(k + GD - 1);
    rd(f1, k + 1);
    mm(f0);
    wait_v<3 * (GD - 3)>();
    bar();
    issue(k + GD);
    rd(f0, k + 2);
    mm(f1);
  }
  if (NST & 1) mm(f0);
  wait_v<0>();
  bar_l();
  const unsigned xb = nq * 5120 + l * 16;
  if (kh == 0) {
    *(LAS f32x4*)(xb + 3 * 1024) = acc[3];
    *(LAS f32x4*)(xb + 4 * 1024) = acc[4];
  } else {
    *(LAS f32x4*)(xb) = acc[0];
    *(LAS f32x4*)(xb + 1024) = acc[1];
    *(LAS f32x4*)(xb + 2048) = acc[2];
  }
  bar_l();
  if (kh == 0) {
    acc[0] += *(LAS f32x4*)(xb);
    acc[1] += *(LAS f32x4*)(xb + 1024);
    acc[2] += *(LAS f32x4*)(xb + 2048);
  } else {
    acc[0] = acc[3] + *(LAS f32x4*)(xb + 3 * 1024);
    acc[1] = acc[4] + *(LAS f32x4*)(xb + 4 * 1024);
  }
}

DI void phase_u(const Params& p, int ft, int dp, int w, int l) {
  const rsrc_t rw = mk(p.ws, WS_END), rb1 = mk(p.b1, F * 2);
  const int g = l >> 4, i16 = l & 15, kh = w >> 2, nq = w & 3;
  const u32x2 bb = __builtin_amdgcn_raw_buffer_load_b64(rb1, (ft * 64 + 16 * nq + 4 * g) * 2, 0, 0);
  f32x4 acc[5];
  gemm6480<0>(p, acc, dp, ft * 64, w, l);
  STAMP(6);
  const f32x4 bias = {bflo(bb[0]), bfhi(bb[0]), bflo(bb[1]), bfhi(bb[1])};
  const unsigned ob = AP_OFF + (ft * 8 + 2 * nq + (g >> 1)) * 10240 + (dp * 80 + 48 * kh + i16) * 16 + (g & 1) * 8;
#pragma unroll
  for (int j = 0; j < 3; ++j) {
    if (j == 2 && kh) break;
    float o[4];
#pragma unroll
    for (int r = 0; r < 4; ++r) {
      const float u = acc[j][r] + bias[r];
      const float z = u * (u * u * (-0.044715f * 2.302208198f) - 2.302208198f);
      o[r] = u * __builtin_amdgcn_rcpf(1.f + __builtin_amdgcn_exp2f(z));
    }
    __builtin_amdgcn_raw_buffer_store_b64(u32x2{pk2(o[0], o[1]), pk2(o[2], o[3])}, rw, ob + j * 256, 0, SC1);
  }
}

DI void phase_y(const Params& p, int st, int dp, int w, int l) {
  const rsrc_t rx = mk(p.x, S * D * 2), ro = mk(p.out, S * D * 2), rb2 = mk(p.b2, S * 2);
  const int g = l >> 4, i16 = l & 15, kh = w >> 2, nq = w & 3;
  const int s = st * 64 + 16 * nq + i16;
  const unsigned ob = s * 1280 + (dp * 80 + 48 * kh + 4 * g) * 2;
  u32x2 xv[3];
#pragma unroll
  for (int j = 0; j < 3; ++j) xv[j] = __builtin_amdgcn_raw_buffer_load_b64(rx, (j == 2 && kh) ? OOB : ob + j * 32, 0, 0);
  const float bias = bflo(__builtin_amdgcn_raw_buffer_load_b16(rb2, s * 2, 0, 0));
  f32x4 acc[5];
  gemm6480<1>(p, acc, dp, st * 64, w, l);
  STAMP(9);
#pragma unroll
  for (int j = 0; j < 3; ++j) {
    if (j == 2 && kh) break;
    const float o0 = acc[j][0] + bias + bflo(xv[j][0]), o1 = acc[j][1] + bias + bfhi(xv[j][0]);
    const float o2 = acc[j][2] + bias + bflo(xv[j][1]), o3 = acc[j][3] + bias + bfhi(xv[j][1]);
    __builtin_amdgcn_raw_buffer_store_b64(u32x2{pk2(o0, o1), pk2(o2, o3)}, ro, ob + j * 32, 0, 0);
  }
}

DI void phase_attn(const Params& p, int h, int w, int l) {
  const rsrc_t rw = mk(p.ws, WS_END);
  const int g = l >> 4, i16 = l & 15;
  const int c32 = l & 31;
  u32x4 kf[16], vf[16];
  {
    const unsigned vo = c32 < 20 ? QT_OFF + (32 * w + (l >> 5)) * 30720 + (640 + 20 * h + c32) * 16 : OOB;
#pragma unroll
    for (int j = 0; j < 16; ++j) {
      kf[j] = __builtin_amdgcn_raw_buffer_load_b128(rw, vo, j * 61440, SC1);
      vf[j] = __builtin_amdgcn_raw_buffer_load_b128(rw, vo, j * 61440 + 10240, SC1);
    }
  }
  constexpr unsigned PQ = 32768;
#pragma unroll
  for (int j = 0; j < 5; ++j) {
    const int c = 64 * j + l;
    const unsigned vo = QT_OFF + (32 * w + c / 20) * 30720 + (20 * h + c % 20) * 16;
    dma_sc1(vo, rw, 0, PQ + w * 10240 + j * 1024);
    dma_sc1(vo, rw, 16 * 30720, PQ + w * 10240 + 5120 + j * 1024);
  }
  f32x16 kv;
#pragma unroll
  for (int r = 0; r < 16; ++r) kv[r] = 0.f;
  const unsigned one = c32 == 20 ? 0x3f803f80u : 0u;
#pragma unroll
  for (int j = 0; j < 16; ++j) {
    const u32x4 v = vf[j] | one;
    kv = __builtin_amdgcn_mfma_f32_32x32x16_bf16(__builtin_bit_cast(bf16x8, kf[j]), __builtin_bit_cast(bf16x8, v), kv, 0, 0, 0);
  }
#pragma unroll
  for (int a = 0; a < 3; ++a) *(LAS f32x4*)(w * 3072 + a * 1024 + l * 16) = f32x4{kv[4 * a], kv[4 * a + 1], kv[4 * a + 2], kv[4 * a + 3]};
  bar_l();
  if (w < 3) {
    f32x4 s = *(LAS f32x4*)(w * 1024 + l * 16);
#pragma unroll
    for (int j = 1; j < 8; ++j) s += *(LAS f32x4*)(j * 3072 + w * 1024 + l * 16);
    *(LAS u32x2*)(24576 + w * 512 + l * 8) = u32x2{pk2(s[0], s[1]), pk2(s[2], s[3])};
  }
  wait_v<0>();
  bar_l();
  bf16x8 B[2];
#pragma unroll
  for (int n = 0; n < 2; ++n) {
    const unsigned a0 = 24576 + (g < 3 ? g : 2) * 512 + (16 * n + i16) * 8;
    const u32x2 lo = *(LAS u32x2*)(a0 + (g < 3 ? 0 : 256)), hi = *(LAS u32x2*)(a0 + 256);
    B[n] = __builtin_bit_cast(bf16x8, u32x4{lo[0], lo[1], hi[0], hi[1]});
  }
  const int q = i16 >> 2, pp = l & 3;
  const int d0 = 8 * g + q, d1 = d0 + 4;
  const unsigned t0 = PQ + w * 10240 + pp * 320 + (d0 < 20 ? d0 : q) * 16, t1 = PQ + w * 10240 + pp * 320 + (d1 < 20 ? d1 : q) * 16;
  const unsigned o1 = OT_OFF + (20 * h + i16) * 4096 + (256 * w + 8 * g) * 2;
  const unsigned o2 = i16 < 4 ? o1 + 16 * 4096 : OOB;
#pragma unroll 2
  for (int G = 0; G < 8; ++G) {
    unsigned r1[4], r2[4];
#pragma unroll
    for (int u = 0; u < 2; ++u) {
      const s16x4 lo = trd(t0 + G * 1280 + 8 * u), hi = trd(t1 + G * 1280 + 8 * u);
      const bf16x8 a = __builtin_bit_cast(bf16x8, __builtin_shufflevector(lo, hi, 0, 1, 2, 3, 4, 5, 6, 7));
      const f32x4 z = {0, 0, 0, 0};
      f32x4 n1 = __builtin_amdgcn_mfma_f32_16x16x32_bf16(a, B[0], z, 0, 0, 0);
      f32x4 n2 = __builtin_amdgcn_mfma_f32_16x16x32_bf16(a, B[1], z, 0, 0, 0);
#pragma unroll
      for (int r = 0; r < 4; ++r) {
        const float inv = __builtin_amdgcn_rcpf(dpp<0x154>(n2[r]) + 1e-6f);
        n1[r] *= inv; n2[r] *= inv;
      }
      r1[2 * u] = pk2(n1[0], n1[1]); r1[2 * u + 1] = pk2(n1[2], n1[3]);
      r2[2 * u] = pk2(n2[0], n2[1]); r2[2 * u + 1] = pk2(n2[2], n2[3]);
    }
    __builtin_amdgcn_raw_buffer_store_b128(u32x4{r1[0], r1[1], r1[2], r1[3]}, rw, o1, G * 64, SC1);
    __builtin_amdgcn_raw_buffer_store_b128(u32x4{r2[0], r2[1], r2[2], r2[3]}, rw, o2, G * 64, SC1);
  }
}

template <int PH>
__global__ __launch_bounds__(512, 2) __attribute__((amdgpu_waves_per_eu(2,2))) void fused(Params p) {
  extern __shared__ __attribute__((aligned(16))) char lds[];
  const int w = __builtin_amdgcn_readfirstlane(threadIdx.x >> 6), l = threadIdx.x & 63;
  const int xcd = blockIdx.x & 7, idx = blockIdx.x >> 3;
  if (PH == 0) {
    STAMP(0);
    phase_ln(p, w, l);
    if (idx >= 30) phase_aux(p, (idx - 30) * 8 + xcd, w, l);
    STAMP(1);
  }
  if (PH == 1) {
    STAMP(2);
    if (idx < 30) { const int t = (xcd & 1) * 30 + idx; phase_qkv(p, (xcd >> 1) * 4 + (t & 3), t >> 2, w, l); }
    STAMP(4);
  }
  if (PH == 2) {
    STAMP(5);
    if (idx < 27) { const int t = (xcd >> 1) * 27 + idx; phase_u(p, t >> 2, (xcd & 1) * 4 + (t & 3), w, l); }
    else if (idx < 31) phase_attn(p, (idx - 27) * 8 + xcd, w, l);
    STAMP(7);
  }
  if (PH == 3) {
    STAMP(8);
    phase_y(p, (xcd >> 1) * 8 + (idx >> 2), (xcd & 1) * 4 + (idx & 3), w, l);
    STAMP(10);
  }
}

extern "C" void kernel_launch(void* const* d_in, const int* in_sizes, int n_in, void* d_out, int out_size, void* d_ws, size_t ws_size,
                              hipStream_t stream) {
  Params p;
  p.x = d_in[0]; p.wqkv = d_in[1]; p.wo = d_in[2]; p.w1 = d_in[4]; p.b1 = d_in[5]; p.w2 = d_in[6]; p.b2 = d_in[7];
  p.out = d_out; p.ws = d_ws;
  (void)hipFuncSetAttribute((const void*)fused<0>, hipFuncAttributeMaxDynamicSharedMemorySize, LDS_BYTES);
  (void)hipFuncSetAttribute((const void*)fused<1>, hipFuncAttributeMaxDynamicSharedMemorySize, LDS_BYTES);
  (void)hipFuncSetAttribute((const void*)fused<2>, hipFuncAttributeMaxDynamicSharedMemorySize, LDS_BYTES);
  (void)hipFuncSetAttribute((const void*)fused<3>, hipFuncAttributeMaxDynamicSharedMemorySize, LDS_BYTES);
  fused<0><<<256, 512, LDS_BYTES, stream>>>(p);
  fused<1><<<256, 512, LDS_BYTES, stream>>>(p);
  fused<2><<<256, 512, LDS_BYTES, stream>>>(p);
  fused<3><<<256, 512, LDS_BYTES, stream>>>(p);
}
```

```cpp
#include <hip/hip_runtime.h>
#include <hip/hip_bf16.h>

typedef short s16x4 __attribute__((ext_vector_type(4)));
typedef short s16x8 __attribute__((ext_vector_type(8)));
typedef __bf16 bf16x8 __attribute__((ext_vector_type(8)));
typedef __bf16 bf16x2 __attribute__((ext_vector_type(2)));
typedef float f32x2 __attribute__((ext_vector_type(2)));
typedef float f32x4 __attribute__((ext_vector_type(4)));
typedef float f32x16 __attribute__((ext_vector_type(16)));
typedef unsigned u32x2 __attribute__((ext_vector_type(2)));
typedef unsigned u32x4 __attribute__((ext_vector_type(4)));
typedef __amdgpu_buffer_rsrc_t rsrc_t;
#define LAS __attribute__((address_space(3)))
#define DI __device__ __forceinline__

constexpr int S = 2048, D = 640, F = 1728, N3 = 1920;
constexpr unsigned HT_OFF = 0;
constexpr unsigned AP_OFF = HT_OFF + 256 * 10240;
constexpr unsigned OT_OFF = AP_OFF + 296 * 10240;
constexpr unsigned QT_OFF = OT_OFF + 640 * 4096;
constexpr unsigned ST_OFF = QT_OFF + 256 * 30720;
constexpr unsigned CS_OFF = ST_OFF + 2 * 2048 * 4;
constexpr unsigned FL_OFF = CS_OFF + 2048 * 4;
constexpr unsigned WS_END = FL_OFF + 8192;
constexpr unsigned OOB = 0x7fff0000u;
constexpr int SC1 = 16;
constexpr int LDS_BYTES = 160 * 1024;

struct Params {
  const void *x, *wqkv, *wo, *w1, *b1, *w2, *b2;
  void *out, *ws;
};

DI rsrc_t mk(const void* p, unsigned bytes) { return __builtin_amdgcn_make_buffer_rsrc((void*)p, (short)0, (int)bytes, 0x00020000); }
DI float bflo(unsigned u) { return __builtin_bit_cast(float, u << 16); }
DI float bfhi(unsigned u) { return __builtin_bit_cast(float, u & 0xffff0000u); }
DI unsigned pk2(float a, float b) { bf16x2 v = {(__bf16)a, (__bf16)b}; return __builtin_bit_cast(unsigned, v); }
template <int C> DI float dpp(float v) { return __builtin_bit_cast(float, __builtin_amdgcn_update_dpp(0, __builtin_bit_cast(int, v), C, 0xf, 0xf, true)); }
DI float wave_sum(float v) {
  v += dpp<0xB1>(v);
  v += dpp<0x4E>(v);
  v += dpp<0x141>(v);
  v += dpp<0x140>(v);
  unsigned u = __builtin_bit_cast(unsigned, v);
  auto a = __builtin_amdgcn_permlane16_swap(u, u, false, false);
  v = __builtin_bit_cast(float, (unsigned)a[0]) + __builtin_bit_cast(float, (unsigned)a[1]);
  u = __builtin_bit_cast(unsigned, v);
  auto b = __builtin_amdgcn_permlane32_swap(u, u, false, false);
  return __builtin_bit_cast(float, (unsigned)b[0]) + __builtin_bit_cast(float, (unsigned)b[1]);
}
DI s16x4 trd(unsigned a) { return __builtin_amdgcn_ds_read_tr16_b64_v4i16((LAS s16x4*)a); }
DI bf16x8 trd2(unsigned a, unsigned d) { s16x4 lo = trd(a), hi = trd(a + d); return __builtin_bit_cast(bf16x8, __builtin_shufflevector(lo, hi, 0, 1, 2, 3, 4, 5, 6, 7)); }
DI bf16x8 rd128(unsigned a) { return *(LAS bf16x8*)a; }
DI void bar() { asm volatile("" ::: "memory");
 __builtin_amdgcn_s_barrier();
 asm volatile("" ::: "memory"); }
DI void bar_l() { asm volatile("s_waitcnt lgkmcnt(0)" ::: "memory"); __builtin_amdgcn_s_barrier(); asm volatile("" ::: "memory"); }
template <int N> DI void wait_v() { asm volatile("s_waitcnt vmcnt(%0)" ::"i"(N) : "memory"); }
DI void dma(unsigned voff, rsrc_t r, unsigned soff, unsigned lds) {
  asm volatile("s_mov_b32 m0, %3\n\ts_nop 0\n\tbuffer_load_dwordx4 %0, %1, %2 offen lds" ::"v"(voff), "s"(r), "s"(soff), "s"(lds) : "memory");
}
DI void dma_sc1(unsigned voff, rsrc_t r, unsigned soff, unsigned lds) {
  asm volatile("s_mov_b32 m0, %3\n\ts_nop 0\n\tbuffer_load_dwordx4 %0, %1, %2 offen sc1 lds" ::"v"(voff), "s"(r), "s"(soff), "s"(lds) : "memory");
}

#define STAMP(i)

DI void phase_ln(const Params& p, int w, int l) {
  const rsrc_t rx = mk(p.x, S * D * 2), rw = mk(p.ws, WS_END);
  const int bid = blockIdx.x, row = 8 * bid + w;
  constexpr unsigned LB = 128 * 1024, RS = 1344;
  unsigned u[5];
#pragma unroll
  for (int j = 0; j < 5; ++j) u[j] = __builtin_amdgcn_raw_buffer_load_b32(rx, l * 4 + j * 256, row * 1280, 0);
  float v[10];
  float s = 0.f;
#pragma unroll
  for (int j = 0; j < 5; ++j) { v[2 * j] = bflo(u[j]); v[2 * j + 1] = bfhi(u[j]); s += v[2 * j] + v[2 * j + 1]; }
  const float m = wave_sum(s) * (1.f / D);
  float q = 0.f;
#pragma unroll
  for (int j = 0; j < 10; ++j) { v[j] -= m; q += v[j] * v[j]; }
  const float r = __builtin_amdgcn_rsqf(wave_sum(q) * (1.f / D) + 1e-5f);
  float s2 = 0.f, q2 = 0.f;
#pragma unroll
  for (int j = 0; j < 5; ++j) {
    const unsigned t = pk2(v[2 * j] * r, v[2 * j + 1] * r);
    v[2 * j] = bflo(t); v[2 * j + 1] = bfhi(t);
    s2 += v[2 * j] + v[2 * j + 1]; q2 += v[2 * j] * v[2 * j] + v[2 * j + 1] * v[2 * j + 1];
  }
  const float m2 = wave_sum(s2) * (1.f / D);
  const float var2 = fmaxf(wave_sum(q2) * (1.f / D) - m2 * m2, 0.f);
  const float r2 = __builtin_amdgcn_rsqf(var2 + 1e-5f);
#pragma unroll
  for (int j = 0; j < 5; ++j) *(LAS unsigned*)(LB + w * RS + l * 4 + j * 256) = pk2((v[2 * j] - m2) * r2, (v[2 * j + 1] - m2) * r2);
  {
    const float sv = (l & 1) ? -m * r : r;
    __builtin_amdgcn_raw_buffer_store_b32(__builtin_bit_cast(unsigned, sv), rw, l < 2 ? ST_OFF + l * 8192 + row * 4 : OOB, 0, SC1);
  }
  bar_l();
  const unsigned ta = LB + ((l & 15) >> 2) * RS + (l >> 4) * 32 + (l & 3) * 8;
  {
    bf16x8 c = trd2(ta + w * 128, 4 * RS);
    __builtin_amdgcn_raw_buffer_store_b128(__builtin_bit_cast(u32x4, c), rw, HT_OFF + l * 16, bid * 10240 + w * 1024, SC1);
  }
  if (w < 2) {
    bf16x8 c = trd2(ta + (w + 8) * 128, 4 * RS);
    __builtin_amdgcn_raw_buffer_store_b128(__builtin_bit_cast(u32x4, c), rw, HT_OFF + l * 16, bid * 10240 + (w + 8) * 1024, SC1);
  }
}

DI void phase_aux(const Params& p, int a, int w, int l) {
  const rsrc_t rw = mk(p.ws, WS_END), rwo = mk(p.wo, D * D * 2), rq = mk(p.wqkv, D * N3 * 2);
  for (int j = w; j < 50; j += 8) dma(l * 16, rwo, a * 51200 + j * 1024, j * 1024);
  const int t = threadIdx.x;
  float acc[8] = {0, 0, 0, 0, 0, 0, 0, 0};
  if (a < 15) {
    u32x4 v[20];
#pragma unroll
    for (int j = 0; j < 20; ++j) v[j] = __builtin_amdgcn_raw_buffer_load_b128(rq, (t >> 4) * 3840 + (t & 15) * 16, a * 256 + j * 32 * 3840, 0);
#pragma unroll
    for (int j = 0; j < 20; ++j)
#pragma unroll
      for (int e = 0; e < 4; ++e) { acc[2 * e] += bflo(v[j][e]); acc[2 * e + 1] += bfhi(v[j][e]); }
  }
  constexpr unsigned CB = 64 * 1024;
  *(LAS f32x4*)(CB + t * 32) = f32x4{acc[0], acc[1], acc[2], acc[3]};
  *(LAS f32x4*)(CB + t * 32 + 16) = f32x4{acc[4], acc[5], acc[6], acc[7]};
  wait_v<0>();
  bar_l();
  if (t < 128 && a < 15) {
    float s = 0.f;
#pragma unroll
    for (int j = 0; j < 32; ++j) s += *(LAS float*)(CB + j * 512 + t * 4);
    __builtin_amdgcn_raw_buffer_store_b32(__builtin_bit_cast(unsigned, s), rw, CS_OFF + (a * 128 + t) * 4, 0, SC1);
  }
  const unsigned ta = ((l & 15) >> 2) * 1280 + (l >> 4) * 32 + (l & 3) * 8;
  for (int j = w; j < 50; j += 8) {
    const int c = j / 10, cb = j % 10;
    bf16x8 v = trd2(ta + c * 10240 + cb * 128, 4 * 1280);
    __builtin_amdgcn_raw_buffer_store_b128(__builtin_bit_cast(u32x4, v), rw, AP_OFF + l * 16, (216 + 5 * a + c) * 10240 + cb * 1024, SC1);
  }
}

DI void phase_qkv(const Params& p, int rp, int ct, int w, int l) {
  const rsrc_t rx = mk(p.x, S * D * 2), rq = mk(p.wqkv, D * N3 * 2), rw = mk(p.ws, WS_END);
  const int g = l >> 4, i16 = l & 15;
  const int row0 = rp * 128, col0 = ct * 128;
  constexpr unsigned SLOT = 32768;
  unsigned vx[2], vw[2];
#pragma unroll
  for (int u = 0; u < 2; ++u) {
    const int j = 2 * w + u;
    vx[u] = (row0 + 8 * j + (l >> 3)) * 1280 + (((l & 7) ^ ((j & 3) * 2 + ((l >> 4) & 1))) * 16);
    const int f = g | ((w & 1) << 2);
    vw[u] = (4 * j + g) * 3840 + col0 * 2 + (((((i16 >> 1) ^ f) << 1) | (i16 & 1)) * 16);
  }
  auto issue = [&](int k) {
    const unsigned sb = (k & 3) * SLOT + w * 2048;
    dma(vx[0], rx, k * 128, sb);
    dma(vx[1], rx, k * 128, sb + 1024);
    dma(vw[0], rq, k * (64 * 3840), sb + 16384);
    dma(vw[1], rq, k * (64 * 3840), sb + 16384 + 1024);
  };
  issue(0); issue(1); issue(2);
  const int mh = w >> 2, nq = w & 3;
  unsigned ra[2], rb[2];
#pragma unroll
  for (int ks = 0; ks < 2; ++ks) ra[ks] = (64 * mh + 8 * (i16 >> 2) + (i16 & 3)) * 128 + (((4 * ks + g) ^ (i16 >> 1)) * 16);
#pragma unroll
  for (int n = 0; n < 2; ++n) {
    const int q = i16 >> 2, pp = l & 3, f = q | ((g & 1) << 2);
    rb[n] = 16384 + (8 * g + q) * 256 + (((2 * nq + n) ^ f) * 32) + 8 * pp;
  }
  f32x4 acc[4][2];
#pragma unroll
  for (int m = 0; m < 4; ++m) { acc[m][0] = f32x4{0, 0, 0, 0}; acc[m][1] = f32x4{0, 0, 0, 0}; }
#pragma unroll 1
  for (int k = 0; k < 10; ++k) {
    if (k < 8) wait_v<8>(); else if (k == 8) wait_v<4>(); else wait_v<0>();
    bar();
    if (k + 3 < 10) issue(k + 3);
    const unsigned sb = (k & 3) * SLOT;
#pragma unroll
    for (int ks = 0; ks < 2; ++ks) {
      bf16x8 a[4], b[2];
#pragma unroll
      for (int n = 0; n < 2; ++n) b[n] = trd2(sb + rb[n] + ks * 8192, 1024);
#pragma unroll
      for (int m = 0; m < 4; ++m) a[m] = rd128(sb + ra[ks] + (m >> 1) * 4096 + (m & 1) * 512);
#pragma unroll
      for (int m = 0; m < 4; ++m)
#pragma unroll
        for (int n = 0; n < 2; ++n) acc[m][n] = __builtin_amdgcn_mfma_f32_16x16x32_bf16(a[m], b[n], acc[m][n], 0, 0, 0);
    }
  }
  STAMP(3);
  float cs[2];
#pragma unroll
  for (int n = 0; n < 2; ++n) cs[n] = __builtin_bit_cast(float, __builtin_amdgcn_raw_buffer_load_b32(rw, CS_OFF + (col0 + 32 * nq + 16 * n + i16) * 4, 0, SC1));
  f32x4 rr[4], mr[4];
#pragma unroll
  for (int m = 0; m < 4; ++m) {
    const unsigned o = ST_OFF + (row0 + 64 * mh + 32 * (m >> 1) + 8 * g + 4 * (m & 1)) * 4;
    rr[m] = __builtin_bit_cast(f32x4, __builtin_amdgcn_raw_buffer_load_b128(rw, o, 0, SC1));
    mr[m] = __builtin_bit_cast(f32x4, __builtin_amdgcn_raw_buffer_load_b128(rw, o + 8192, 0, SC1));
  }
#pragma unroll
  for (int m = 0; m < 4; ++m)
#pragma unroll
    for (int n = 0; n < 2; ++n) acc[m][n] = acc[m][n] * rr[m] + mr[m] * cs[n];
  if (ct < 10) {
#pragma unroll
    for (int m = 0; m < 4; ++m)
#pragma unroll
      for (int n = 0; n < 2; ++n)
#pragma unroll
        for (int r = 0; r < 4; ++r) {
          const float t = acc[m][n][r];
          acc[m][n][r] = (fmaxf(t, 0.f) + 1e-6f) + __builtin_amdgcn_exp2f(fminf(t * 1.44269504088896f, 0.f));
        }
  }
#pragma unroll
  for (int P = 0; P < 2; ++P)
#pragma unroll
    for (int n = 0; n < 2; ++n) {
      const f32x4 lo = acc[2 * P][n], hi = acc[2 * P + 1][n];
      u32x4 o = {pk2(lo[0], lo[1]), pk2(lo[2], lo[3]), pk2(hi[0], hi[1]), pk2(hi[2], hi[3])};
      const unsigned chunk = (row0 + 64 * mh + 32 * P) / 8 + g;
      __builtin_amdgcn_raw_buffer_store_b128(o, rw, QT_OFF + chunk * 30720 + (col0 + 32 * nq + 16 * n + i16) * 16, 0, SC1);
    }
}

#define SCA "sc1"
#define SCB "sc1"
struct Frag { bf16x8 a[5]; bf16x8 b; };
constexpr unsigned GSLOT = 18432;
constexpr int GD = 8;
DI void g_issue(bool real, unsigned va, rsrc_t ra, unsigned sa, unsigned vb, rsrc_t rb, unsigned sb, unsigned ldsA, unsigned dB) {
  unsigned long long m1 = real ? ~0ull : 1ull, m2 = real ? 0xffffull : 1ull, m3 = m1;
  asm volatile(
      "s_mov_b64 exec, %8\n\ts_mov_b32 m0, %6\n\ts_nop 0\n\t"
      "buffer_load_dwordx4 %0, %1, %2 offen " SCA " lds\n\t"
      "s_mov_b64 exec, %9\n\t"
      "buffer_load_dwordx4 %0, %1, %2 offen offset:1024 " SCA " lds\n\t"
      "s_mov_b64 exec, %10\n\ts_add_u32 m0, %6, %7\n\ts_nop 0\n\t"
      "buffer_load_dwordx4 %3, %4, %5 offen " SCB " lds\n\t"
      "s_mov_b64 exec, -1"
      ::"v"(va), "s"(ra), "s"(sa), "v"(vb), "s"(rb), "s"(sb), "s"(ldsA), "s"(dB), "s"(m1), "s"(m2), "s"(m3) : "memory", "scc");
}
template <int MODE>
DI void gemm6480(const Params& p, f32x4 (&acc)[5], int dp, int col0, int w, int l) {
  constexpr int NST = MODE == 0 ? 32 : 37;
  constexpr unsigned RSB = MODE == 0 ? 3456 : 4096;
  const rsrc_t rw = mk(p.ws, WS_END);
  const rsrc_t rb0 = MODE == 0 ? mk(p.w1, S * F * 2) : mk(p.w2, S * F * 2);
  const int g = l >> 4, i16 = l & 15;
  const unsigned va = (MODE == 0 ? HT_OFF : AP_OFF) + w * 10240 + dp * 1280 + l * 16;
  const int fb = ((l >> 4) & 1) | ((w & 1) << 1);
  const unsigned vb = (8 * w + (l >> 3)) * RSB + col0 * 2 + ((((((l & 7) >> 1) ^ fb) << 1) | (l & 1)) * 16);
  const unsigned dB = 10240 + w * 1024 - w * 1280;
  auto issue = [&](int k) {
    const unsigned ldsA = (k & (GD - 1)) * GSLOT + w * 1280;
    if (MODE == 1 && k >= 27) g_issue(k < NST, va, rw, k * 81920, vb, rw, OT_OFF + (k - 27) * (64 * RSB), ldsA, dB);
    else g_issue(k < NST, va, rw, k * 81920, vb, rb0, k * (64 * RSB), ldsA, dB);
  };
#pragma unroll 1
  for (int k = 0; k < GD - 1; ++k) issue(k);
  const int kh = w >> 2, nq = w & 3;
  const unsigned la = (4 * kh + g) * 1280 + i16 * 16;
  const int q = i16 >> 2;
  const unsigned lb = 10240 + (32 * kh + 8 * g + q) * 128 + ((nq ^ (((q >> 1) & 1) | ((g & 1) << 1))) * 32) + 8 * (l & 3);
  auto rd = [&](Frag& f, int k) {
    const unsigned sb = (k & (GD - 1)) * GSLOT;
    f.b = trd2(sb + lb, 512);
#pragma unroll
    for (int j = 0; j < 5; ++j) f.a[j] = rd128(sb + la + j * 256);
  };
  auto mm = [&](const Frag& f) {
#pragma unroll
    for (int j = 0; j < 5; ++j)
      acc[j] = MODE == 0 ? __builtin_amdgcn_mfma_f32_16x16x32_bf16(f.b, f.a[j], acc[j], 0, 0, 0)
                         : __builtin_amdgcn_mfma_f32_16x16x32_bf16(f.a[j], f.b, acc[j], 0, 0, 0);
  };
#pragma unroll
  for (int j = 0; j < 5; ++j) acc[j] = f32x4{0, 0, 0, 0};
  Frag f0, f1;
  wait_v<3 * (GD - 2)>();
  bar();
  rd(f0, 0);
#pragma unroll 1
  for (int k = 0; k + 1 < NST; k += 2) {
    wait_v<3 * (GD - 3)>();
    bar();
    issue(k + GD - 1);
    rd(f1, k + 1);
    mm(f0);
    wait_v<3 * (GD - 3)>();
    bar();
    issue(k + GD);
    rd(f0, k + 2);
    mm(f1);
  }
  if (NST & 1) mm(f0);
  wait_v<0>();
  bar_l();
  const unsigned xb = nq * 5120 + l * 16;
  if (kh == 0) {
    *(LAS f32x4*)(xb + 3 * 1024) = acc[3];
    *(LAS f32x4*)(xb + 4 * 1024) = acc[4];
  } else {
    *(LAS f32x4*)(xb) = acc[0];
    *(LAS f32x4*)(xb + 1024) = acc[1];
    *(LAS f32x4*)(xb + 2048) = acc[2];
  }
  bar_l();
  if (kh == 0) {
    acc[0] += *(LAS f32x4*)(xb);
    acc[1] += *(LAS f32x4*)(xb + 1024);
    acc[2] += *(LAS f32x4*)(xb + 2048);
  } else {
    acc[0] = acc[3] + *(LAS f32x4*)(xb + 3 * 1024);
    acc[1] = acc[4] + *(LAS f32x4*)(xb + 4 * 1024);
  }
}

DI void phase_u(const Params& p, int ft, int dp, int w, int l) {
  const rsrc_t rw = mk(p.ws, WS_END), rb1 = mk(p.b1, F * 2);
  const int g = l >> 4, i16 = l & 15, kh = w >> 2, nq = w & 3;
  const u32x2 bb = __builtin_amdgcn_raw_buffer_load_b64(rb1, (ft * 64 + 16 * nq + 4 * g) * 2, 0, 0);
  f32x4 acc[5];
  gemm6480<0>(p, acc, dp, ft * 64, w, l);
  STAMP(6);
  const f32x4 bias = {bflo(bb[0]), bfhi(bb[0]), bflo(bb[1]), bfhi(bb[1])};
  const unsigned ob = AP_OFF + (ft * 8 + 2 * nq + (g >> 1)) * 10240 + (dp * 80 + 48 * kh + i16) * 16 + (g & 1) * 8;
#pragma unroll
  for (int j = 0; j < 3; ++j) {
    if (j == 2 && kh) break;
    float o[4];
#pragma unroll
    for (int r = 0; r < 4; ++r) {
      const float u = acc[j][r] + bias[r];
      const float z = u * (u * u * (-0.044715f * 2.302208198f) - 2.302208198f);
      o[r] = u * __builtin_amdgcn_rcpf(1.f + __builtin_amdgcn_exp2f(z));
    }
    __builtin_amdgcn_raw_buffer_store_b64(u32x2{pk2(o[0], o[1]), pk2(o[2], o[3])}, rw, ob + j * 256, 0, SC1);
  }
}

DI void phase_y(const Params& p, int st, int dp, int w, int l) {
  const rsrc_t rx = mk(p.x, S * D * 2), ro = mk(p.out, S * D * 2), rb2 = mk(p.b2, S * 2);
  const int g = l >> 4, i16 = l & 15, kh = w >> 2, nq = w & 3;
  const int s = st * 64 + 16 * nq + i16;
  const unsigned ob = s * 1280 + (dp * 80 + 48 * kh + 4 * g) * 2;
  u32x2 xv[3];
#pragma unroll
  for (int j = 0; j < 3; ++j) xv[j] = __builtin_amdgcn_raw_buffer_load_b64(rx, (j == 2 && kh) ? OOB : ob + j * 32, 0, 0);
  const float bias = bflo(__builtin_amdgcn_raw_buffer_load_b16(rb2, s * 2, 0, 0));
  f32x4 acc[5];
  gemm6480<1>(p, acc, dp, st * 64, w, l);
  STAMP(9);
#pragma unroll
  for (int j = 0; j < 3; ++j) {
    if (j == 2 && kh) break;
    const float o0 = acc[j][0] + bias + bflo(xv[j][0]), o1 = acc[j][1] + bias + bfhi(xv[j][0]);
    const float o2 = acc[j][2] + bias + bflo(xv[j][1]), o3 = acc[j][3] + bias + bfhi(xv[j][1]);
    __builtin_amdgcn_raw_buffer_store_b64(u32x2{pk2(o0, o1), pk2(o2, o3)}, ro, ob + j * 32, 0, 0);
  }
}

DI void phase_attn(const Params& p, int h, int w, int l) {
  const rsrc_t rw = mk(p.ws, WS_END);
  const int g = l >> 4, i16 = l & 15;
  const int c32 = l & 31;
  u32x4 kf[16], vf[16];
  {
    const unsigned vo = c32 < 20 ? QT_OFF + (32 * w + (l >> 5)) * 30720 + (640 + 20 * h + c32) * 16 : OOB;
#pragma unroll
    for (int j = 0; j < 16; ++j) {
      kf[j] = __builtin_amdgcn_raw_buffer_load_b128(rw, vo, j * 61440, SC1);
      vf[j] = __builtin_amdgcn_raw_buffer_load_b128(rw, vo, j * 61440 + 10240, SC1);
    }
  }
  constexpr unsigned PQ = 32768;
#pragma unroll
  for (int j = 0; j < 5; ++j) {
    const int c = 64 * j + l;
    const unsigned vo = QT_OFF + (32 * w + c / 20) * 30720 + (20 * h + c % 20) * 16;
    dma_sc1(vo, rw, 0, PQ + w * 10240 + j * 1024);
    dma_sc1(vo, rw, 16 * 30720, PQ + w * 10240 + 5120 + j * 1024);
  }
  f32x16 kv;
#pragma unroll
  for (int r = 0; r < 16; ++r) kv[r] = 0.f;
  const unsigned one = c32 == 20 ? 0x3f803f80u : 0u;
#pragma unroll
  for (int j = 0; j < 16; ++j) {
    const u32x4 v = vf[j] | one;
    kv = __builtin_amdgcn_mfma_f32_32x32x16_bf16(__builtin_bit_cast(bf16x8, kf[j]), __builtin_bit_cast(bf16x8, v), kv, 0, 0, 0);
  }
#pragma unroll
  for (int a = 0; a < 3; ++a) *(LAS f32x4*)(w * 3072 + a * 1024 + l * 16) = f32x4{kv[4 * a], kv[4 * a + 1], kv[4 * a + 2], kv[4 * a + 3]};
  bar_l();
  if (w < 3) {
    f32x4 s = *(LAS f32x4*)(w * 1024 + l * 16);
#pragma unroll
    for (int j = 1; j < 8; ++j) s += *(LAS f32x4*)(j * 3072 + w * 1024 + l * 16);
    *(LAS u32x2*)(24576 + w * 512 + l * 8) = u32x2{pk2(s[0], s[1]), pk2(s[2], s[3])};
  }
  wait_v<0>();
  bar_l();
  bf16x8 B[2];
#pragma unroll
  for (int n = 0; n < 2; ++n) {
    const unsigned a0 = 24576 + (g < 3 ? g : 2) * 512 + (16 * n + i16) * 8;
    const u32x2 lo = *(LAS u32x2*)(a0 + (g < 3 ? 0 : 256)), hi = *(LAS u32x2*)(a0 + 256);
    B[n] = __builtin_bit_cast(bf16x8, u32x4{lo[0], lo[1], hi[0], hi[1]});
  }
  const int q = i16 >> 2, pp = l & 3;
  const int d0 = 8 * g + q, d1 = d0 + 4;
  const unsigned t0 = PQ + w * 10240 + pp * 320 + (d0 < 20 ? d0 : q) * 16, t1 = PQ + w * 10240 + pp * 320 + (d1 < 20 ? d1 : q) * 16;
  const unsigned o1 = OT_OFF + (20 * h + i16) * 4096 + (256 * w + 8 * g) * 2;
  const unsigned o2 = i16 < 4 ? o1 + 16 * 4096 : OOB;
#pragma unroll 2
  for (int G = 0; G < 8; ++G) {
    unsigned r1[4], r2[4];
#pragma unroll
    for (int u = 0; u < 2; ++u) {
      const s16x4 lo = trd(t0 + G * 1280 + 8 * u), hi = trd(t1 + G * 1280 + 8 * u);
      const bf16x8 a = __builtin_bit_cast(bf16x8, __builtin_shufflevector(lo, hi, 0, 1, 2, 3, 4, 5, 6, 7));
      const f32x4 z = {0, 0, 0, 0};
      f32x4 n1 = __builtin_amdgcn_mfma_f32_16x16x32_bf16(a, B[0], z, 0, 0, 0);
      f32x4 n2 = __builtin_amdgcn_mfma_f32_16x16x32_bf16(a, B[1], z, 0, 0, 0);
#pragma unroll
      for (int r = 0; r < 4; ++r) {
        const float inv = __builtin_amdgcn_rcpf(dpp<0x154>(n2[r]) + 1e-6f);
        n1[r] *= inv; n2[r] *= inv;
      }
      r1[2 * u] = pk2(n1[0], n1[1]); r1[2 * u + 1] = pk2(n1[2], n1[3]);
      r2[2 * u] = pk2(n2[0], n2[1]); r2[2 * u + 1] = pk2(n2[2], n2[3]);
    }
    __builtin_amdgcn_raw_buffer_store_b128(u32x4{r1[0], r1[1], r1[2], r1[3]}, rw, o1, G * 64, SC1);
    __builtin_amdgcn_raw_buffer_store_b128(u32x4{r2[0], r2[1], r2[2], r2[3]}, rw, o2, G * 64, SC1);
  }
}

template <int PH>
__global__ __launch_bounds__(512, 2) __attribute__((amdgpu_waves_per_eu(2,2))) void fused(Params p) {
  extern __shared__ __attribute__((aligned(16))) char lds[];
  const int w = __builtin_amdgcn_readfirstlane(threadIdx.x >> 6), l = threadIdx.x & 63;
  const int xcd = blockIdx.x & 7, idx = blockIdx.x >> 3;
  if (PH == 0) {
    STAMP(0);
    phase_ln(p, w, l);
    if (idx >= 30) phase_aux(p, (idx - 30) * 8 + xcd, w, l);
    STAMP(1);
  }
  if (PH == 1) {
    STAMP(2);
    if (idx < 30) { const int t = (xcd & 1) * 30 + idx; phase_qkv(p, (xcd >> 1) * 4 + (t & 3), t >> 2, w, l); }
    STAMP(4);
  }
  if (PH == 2) {
    STAMP(5);
    if (idx < 27) { const int t = (xcd >> 1) * 27 + idx; phase_u(p, t >> 2, (xcd & 1) * 4 + (t & 3), w, l); }
    else if (idx < 31) phase_attn(p, (idx - 27) * 8 + xcd, w, l);
    STAMP(7);
  }
  if (PH == 3) {
    STAMP(8);
    phase_y(p, (xcd >> 1) * 8 + (idx >> 2), (xcd & 1) * 4 + (idx & 3), w, l);
    STAMP(10);
  }
}

template <int POL>
__global__ __launch_bounds__(512) void probe(const char* a, const char* b, char* wsp) {
  unsigned long long r0 = __builtin_amdgcn_s_memrealtime();
  const int t = blockIdx.x * 512 + threadIdx.x;
  unsigned v = 0;
  const char* p = t < 55296 ? a + (size_t)t * 128 : (t < 110592 ? b + (size_t)(t - 55296) * 128 : a);
  if (POL == 2) p = wsp + (size_t)t * 128;
  if (POL == 3) {
    u32x4 z = {1u,2u,3u,4u};
    char* q = wsp + (size_t)t * 16;
    for (int i = 0; i < 8; ++i) asm volatile("global_store_dwordx4 %0, %1, off sc1\n s_nop 1" :: "v"(q + (size_t)i * 2097152), "v"(z) : "memory");
    asm volatile("s_waitcnt vmcnt(0)" ::: "memory");
  }
  else if (POL == 1) asm volatile("global_load_dword %0, %1, off nt\n s_waitcnt vmcnt(0)" : "=&v"(v) : "v"(p) : "memory");
  else asm volatile("global_load_dword %0, %1, off\n s_waitcnt vmcnt(0)" : "=&v"(v) : "v"(p) : "memory");
  __syncthreads();
  unsigned long long r1 = __builtin_amdgcn_s_memrealtime();
  unsigned long long until = r0 + 20 * (r1 - r0);
  while (__builtin_amdgcn_s_memrealtime() < until) __builtin_amdgcn_s_sleep(4);
}

extern "C" void kernel_launch(void* const* d_in, const int* in_sizes, int n_in, void* d_out, int out_size, void* d_ws, size_t ws_size,
                              hipStream_t stream) {
  Params p;
  p.x = d_in[0]; p.wqkv = d_in[1]; p.wo = d_in[2]; p.w1 = d_in[4]; p.b1 = d_in[5]; p.w2 = d_in[6]; p.b2 = d_in[7];
  p.out = d_out; p.ws = d_ws;
  (void)hipFuncSetAttribute((const void*)fused<0>, hipFuncAttributeMaxDynamicSharedMemorySize, LDS_BYTES);
  (void)hipFuncSetAttribute((const void*)fused<1>, hipFuncAttributeMaxDynamicSharedMemorySize, LDS_BYTES);
  (void)hipFuncSetAttribute((const void*)fused<2>, hipFuncAttributeMaxDynamicSharedMemorySize, LDS_BYTES);
  (void)hipFuncSetAttribute((const void*)fused<3>, hipFuncAttributeMaxDynamicSharedMemorySize, LDS_BYTES);
  probe<0><<<256, 512, 0, stream>>>((const char*)p.w1, (const char*)p.w2, (char*)d_ws + 0);
  fused<0><<<256, 512, LDS_BYTES, stream>>>(p);
  fused<1><<<256, 512, LDS_BYTES, stream>>>(p);
  fused<2><<<256, 512, LDS_BYTES, stream>>>(p);
  fused<3><<<256, 512, LDS_BYTES, stream>>>(p);
}
```

```cpp
#include <hip/hip_runtime.h>
#include <hip/hip_bf16.h>

typedef short s16x4 __attribute__((ext_vector_type(4)));
typedef __bf16 bf16x8 __attribute__((ext_vector_type(8)));
typedef __bf16 bf16x2 __attribute__((ext_vector_type(2)));
typedef float f32x2 __attribute__((ext_vector_type(2)));
typedef float f32x4 __attribute__((ext_vector_type(4)));
typedef float f32x16 __attribute__((ext_vector_type(16)));
typedef unsigned u32x2 __attribute__((ext_vector_type(2)));
typedef unsigned u32x8 __attribute__((ext_vector_type(8)));
typedef unsigned u32x16 __attribute__((ext_vector_type(16)));
typedef unsigned u32x4 __attribute__((ext_vector_type(4)));
typedef __amdgpu_buffer_rsrc_t rsrc_t;
#define LAS __attribute__((address_space(3)))
#define DI __device__ __forceinline__
#pragma clang diagnostic ignored "-Wint-to-pointer-cast"

constexpr int S = 2048, D = 640, F = 1728, N3 = 1920;
constexpr unsigned EP_OFF = 0;
constexpr unsigned F1_OFF = 1024;
constexpr unsigned F2_OFF = F1_OFF + 256 * 64;
constexpr unsigned F3_OFF = F2_OFF + 256 * 64;
constexpr unsigned F5_OFF = F3_OFF + 256 * 64;
constexpr unsigned F4_OFF = 65536;
constexpr unsigned ST_OFF = F4_OFF + 1024 * 64;
constexpr unsigned CS_OFF = ST_OFF + 256 * 64;
constexpr unsigned KV_OFF = CS_OFF + 2048 * 8;
constexpr unsigned HT_OFF = KV_OFF + 32 * 3072;
constexpr unsigned GP_OFF = HT_OFF + 8 * 327680;
constexpr unsigned OC_OFF = GP_OFF + 10 * 221184;
constexpr unsigned AO_OFF = OC_OFF + 160 * 16384;
constexpr unsigned QT_OFF = AO_OFF + 80 * 32768;
constexpr unsigned WS_END = QT_OFF + 96 * 81920;
constexpr unsigned OOB = 0x7fff0000u;
constexpr int SC1 = 16;
constexpr int LDS_BYTES = 160 * 1024;

struct Desc { const void* p; unsigned bytes, cfg; };
struct Params { Desc x, wqkv, ws, w1, w2, wo, b1, b2, out; };
struct Rs { rsrc_t x, wq, ws, w1, w2, wo, b1, b2, out; };
constexpr unsigned USLOT = 36864;
constexpr unsigned LNB = 0;
constexpr unsigned QB = 30720;
constexpr unsigned SB = QB + 131072;
#define PIN(x) asm volatile("" : "+v"(x))

DI float asf(unsigned u) { return __builtin_bit_cast(float, u); }
DI float bflo(unsigned u) { return __builtin_bit_cast(float, u << 16); }
DI float bfhi(unsigned u) { return __builtin_bit_cast(float, u & 0xffff0000u); }
DI unsigned pk2(float a, float b) { bf16x2 v = {(__bf16)a, (__bf16)b}; return __builtin_bit_cast(unsigned, v); }
template <int C> DI float dpp(float v) { return __builtin_bit_cast(float, __builtin_amdgcn_update_dpp(0, __builtin_bit_cast(int, v), C, 0xf, 0xf, true)); }
DI float vmax(float a, float b) { float r; asm("v_max_f32 %0, %1, %2" : "=v"(r) : "v"(a), "v"(b)); return r; }
DI f32x4 opaque4(float c) { f32x2 v = {c, c}; PIN(v); return __builtin_shufflevector(v, v, 0, 1, 0, 1); }
#define SCHED() __builtin_amdgcn_sched_barrier(0)
DI u32x4 swap16(unsigned ax, unsigned ay, unsigned bx, unsigned by) {
  auto x = __builtin_amdgcn_permlane16_swap(ax, bx, false, false);
  auto y = __builtin_amdgcn_permlane16_swap(ay, by, false, false);
  return u32x4{x[0], y[0], x[1], y[1]};
}
DI s16x4 trd(unsigned a) { return __builtin_amdgcn_ds_read_tr16_b64_v4i16((LAS s16x4*)a); }
DI bf16x8 trd2(unsigned a, unsigned d) { s16x4 lo = trd(a), hi = trd(a + d); return __builtin_bit_cast(bf16x8, __builtin_shufflevector(lo, hi, 0, 1, 2, 3, 4, 5, 6, 7)); }
DI void bar() { asm volatile("s_barrier" ::: "memory"); }
DI void bar_l() { asm volatile("s_waitcnt lgkmcnt(0)\n\ts_barrier" ::: "memory"); }
template <int N> DI void wait_v() { asm volatile("s_waitcnt vmcnt(%0)" ::"i"(N) : "memory"); }
template <int N> DI void bar_v() { asm volatile("s_waitcnt vmcnt(%0)\n\ts_barrier" ::"i"(N) : "memory"); }
DI void dma_sc1(unsigned voff, rsrc_t r, unsigned soff, unsigned lds) {
  asm volatile("s_mov_b32 m0, %3\n\ts_nop 0\n\tbuffer_load_dwordx4 %0, %1, %2 offen sc1 lds" ::"v"(voff), "s"(r), "s"(soff), "s"(lds) : "memory");
}
DI void flag(rsrc_t rw, unsigned off, unsigned tag, int l) { __builtin_amdgcn_raw_buffer_store_b32(tag, rw, l == 0 ? off : OOB, 0, SC1); }
template <int N, int SL>
DI void poll(rsrc_t rw, const unsigned (&off)[N], unsigned tag) {
#pragma unroll 1
  for (unsigned spins = 0; spins < (1u << 16); ++spins) {
    bool ok = true;
#pragma unroll
    for (int i = 0; i < N; ++i) ok &= __builtin_amdgcn_raw_buffer_load_b32(rw, off[i], 0, SC1) == tag || off[i] == OOB;
    asm volatile("" ::: "memory");
    if (__all(ok)) break;
    if (SL) __builtin_amdgcn_s_sleep(SL);
  }
  asm volatile("" ::: "memory");
}
template <int SL> DI void poll1(rsrc_t rw, unsigned off, unsigned tag) { const unsigned o[1] = {off}; poll<1, SL>(rw, o, tag); }
#define HK1 8
#define HK2 14
#define PSL 0
#define PFC 3
#define OAUX 16
#define PFA 1
#define PFB 3
#define U_LOOP \
      "v_add_u32 v120, %[cur], %[la]\n\t" \
      "v_add_u32 v121, %[cur], %[lb]\n\t" \
      "v_xor_b32 v122, 32, v121\n\t" \
      "ds_read_b64_tr_b16 v[148:149], v121 offset:0\n\t" \
      "ds_read_b64_tr_b16 v[150:151], v121 offset:512\n\t" \
      "ds_read_b64_tr_b16 v[152:153], v122 offset:0\n\t" \
      "ds_read_b64_tr_b16 v[154:155], v122 offset:512\n\t" \
      "ds_read_b128 v[128:131], v120 offset:0\n\t" \
      "ds_read_b128 v[132:135], v120 offset:256\n\t" \
      "ds_read_b128 v[136:139], v120 offset:512\n\t" \
      "ds_read_b128 v[140:143], v120 offset:768\n\t" \
      "ds_read_b128 v[144:147], v120 offset:1024\n\t" \
      "s_branch 2f\n\t" \
      "1:\n\t" \
      "v_mfma_f32_16x16x32_bf16 %[c0], v[180:183], v[160:163], %[c0]\n\t" \
      "ds_read_b64_tr_b16 v[148:149], v121 offset:0\n\t" \
      "ds_read_b64_tr_b16 v[150:151], v121 offset:512\n\t" \
      "v_mfma_f32_16x16x32_bf16 %[c1], v[184:187], v[160:163], %[c1]\n\t" \
      "ds_read_b64_tr_b16 v[152:153], v122 offset:0\n\t" \
      "v_mfma_f32_16x16x32_bf16 %[c2], v[180:183], v[164:167], %[c2]\n\t" \
      "ds_read_b64_tr_b16 v[154:155], v122 offset:512\n\t" \
      "v_mfma_f32_16x16x32_bf16 %[c3], v[184:187], v[164:167], %[c3]\n\t" \
      "ds_read_b128 v[128:131], v120 offset:0\n\t" \
      "v_mfma_f32_16x16x32_bf16 %[c4], v[180:183], v[168:171], %[c4]\n\t" \
      "ds_read_b128 v[132:135], v120 offset:256\n\t" \
      "v_mfma_f32_16x16x32_bf16 %[c5], v[184:187], v[168:171], %[c5]\n\t" \
      "ds_read_b128 v[136:139], v120 offset:512\n\t" \
      "v_mfma_f32_16x16x32_bf16 %[c6], v[180:183], v[172:175], %[c6]\n\t" \
      "ds_read_b128 v[140:143], v120 offset:768\n\t" \
      "v_mfma_f32_16x16x32_bf16 %[c7], v[184:187], v[172:175], %[c7]\n\t" \
      "ds_read_b128 v[144:147], v120 offset:1024\n\t" \
      "v_mfma_f32_16x16x32_bf16 %[c8], v[180:183], v[176:179], %[c8]\n\t" \
      "s_cmp_eq_u32 %[cnt], %[hk]\n\t" \
      "v_mfma_f32_16x16x32_bf16 %[c9], v[184:187], v[176:179], %[c9]\n\t" \
      "s_cbranch_scc1 7f\n\t8:\n\t" \
      "2:\n\t" \
      "s_waitcnt lgkmcnt(0)\n\t" \
      "v_mfma_f32_16x16x32_bf16 %[c0], v[148:151], v[128:131], %[c0]\n\t" \
      "ds_read_b64_tr_b16 v[180:181], v121 offset:18432\n\t" \
      "ds_read_b64_tr_b16 v[182:183], v121 offset:18944\n\t" \
      "v_mfma_f32_16x16x32_bf16 %[c1], v[152:155], v[128:131], %[c1]\n\t" \
      "ds_read_b64_tr_b16 v[184:185], v122 offset:18432\n\t" \
      "v_mfma_f32_16x16x32_bf16 %[c2], v[148:151], v[132:135], %[c2]\n\t" \
      "ds_read_b64_tr_b16 v[186:187], v122 offset:18944\n\t" \
      "v_mfma_f32_16x16x32_bf16 %[c3], v[152:155], v[132:135], %[c3]\n\t" \
      "ds_read_b128 v[160:163], v120 offset:18432\n\t" \
      "v_mfma_f32_16x16x32_bf16 %[c4], v[148:151], v[136:139], %[c4]\n\t" \
      "ds_read_b128 v[164:167], v120 offset:18688\n\t" \
      "v_mfma_f32_16x16x32_bf16 %[c5], v[152:155], v[136:139], %[c5]\n\t" \
      "ds_read_b128 v[168:171], v120 offset:18944\n\t" \
      "v_mfma_f32_16x16x32_bf16 %[c6], v[148:151], v[140:143], %[c6]\n\t" \
      "ds_read_b128 v[172:175], v120 offset:19200\n\t" \
      "v_mfma_f32_16x16x32_bf16 %[c7], v[152:155], v[140:143], %[c7]\n\t" \
      "ds_read_b128 v[176:179], v120 offset:19456\n\t" \
      "v_mfma_f32_16x16x32_bf16 %[c8], v[148:151], v[144:147], %[c8]\n\t" \
      "s_add_u32 %[cur], %[cur], 36864\n\t" \
      "s_cmp_eq_u32 %[cur], 147456\n\t" \
      "s_cselect_b32 %[cur], 0, %[cur]\n\t" \
      "v_add_u32 v120, %[cur], %[la]\n\t" \
      "v_mfma_f32_16x16x32_bf16 %[c9], v[152:155], v[144:147], %[c9]\n\t" \
      "v_add_u32 v121, %[cur], %[lb]\n\t" \
      "v_xor_b32 v122, 32, v121\n\t" \
      "s_add_i32 %[cnt], %[cnt], -1\n\t" \
      "s_cmp_lg_u32 %[cnt], %[hk]\n\t" \
      "s_cbranch_scc1 5f\n\t" \
      "s_waitcnt vmcnt(0)\n\t" \
      "5:\n\t" \
      "s_cmp_lg_u32 %[cnt], 0\n\t" \
      "s_waitcnt lgkmcnt(0)\n\t" \
      "s_cbranch_scc0 4f\n\t" \
      "s_barrier\n\t" \
      "s_branch 1b\n\t" \
      "4:\n\t" \
      "v_mfma_f32_16x16x32_bf16 %[c1], v[184:187], v[160:163], %[c1]\n\t" \
      "v_mfma_f32_16x16x32_bf16 %[c3], v[184:187], v[164:167], %[c3]\n\t" \
      "v_mfma_f32_16x16x32_bf16 %[c5], v[184:187], v[168:171], %[c5]\n\t" \
      "v_mfma_f32_16x16x32_bf16 %[c7], v[184:187], v[172:175], %[c7]\n\t" \
      "v_mfma_f32_16x16x32_bf16 %[c9], v[184:187], v[176:179], %[c9]\n\t" \
      "v_subrev_u32 v122, %[k9], v120\n\t" \
      "v_subrev_u32 v123, %[k9], v121\n\t" \
      "s_nop 5\n\t" \
      "ds_write_b128 %[xw], %[c1] offset:0\n\t" \
      "ds_write_b128 %[xw], %[c3] offset:1024\n\t" \
      "ds_write_b128 %[xw], %[c5] offset:2048\n\t" \
      "ds_write_b128 %[xw], %[c7] offset:3072\n\t" \
      "ds_write_b128 %[xw], %[c9] offset:4096\n\t" \
      "s_waitcnt lgkmcnt(0)\n\t" \
      "s_barrier\n\t" \
      "v_mfma_f32_16x16x32_bf16 %[c0], v[180:183], v[160:163], %[c0]\n\t" \
      "ds_read_b64_tr_b16 v[116:117], v123 offset:0\n\t" \
      "ds_read_b64_tr_b16 v[118:119], v123 offset:512\n\t" \
      "v_mfma_f32_16x16x32_bf16 %[c2], v[180:183], v[164:167], %[c2]\n\t" \
      "ds_read_b128 v[96:99], v122 offset:0\n\t" \
      "ds_read_b128 v[100:103], v122 offset:256\n\t" \
      "v_mfma_f32_16x16x32_bf16 %[c4], v[180:183], v[168:171], %[c4]\n\t" \
      "ds_read_b128 v[104:107], v122 offset:512\n\t" \
      "ds_read_b128 v[108:111], v122 offset:768\n\t" \
      "v_mfma_f32_16x16x32_bf16 %[c6], v[180:183], v[172:175], %[c6]\n\t" \
      "ds_read_b128 v[112:115], v122 offset:1024\n\t" \
      "ds_read_b64_tr_b16 v[148:149], v123 offset:9216\n\t" \
      "v_mfma_f32_16x16x32_bf16 %[c8], v[180:183], v[176:179], %[c8]\n\t" \
      "ds_read_b64_tr_b16 v[150:151], v123 offset:9728\n\t" \
      "ds_read_b128 v[128:131], v122 offset:9216\n\t" \
      "ds_read_b128 v[132:135], v122 offset:9472\n\t" \
      "ds_read_b128 v[136:139], v122 offset:9728\n\t" \
      "ds_read_b128 v[140:143], v122 offset:9984\n\t" \
      "ds_read_b128 v[144:147], v122 offset:10240\n\t" \
      "s_waitcnt lgkmcnt(7)\n\t" \
      "v_mfma_f32_16x16x32_bf16 %[c0], v[116:119], v[96:99], %[c0]\n\t" \
      "ds_read_b64_tr_b16 v[176:177], v123 offset:18432\n\t" \
      "ds_read_b64_tr_b16 v[178:179], v123 offset:18944\n\t" \
      "v_mfma_f32_16x16x32_bf16 %[c2], v[116:119], v[100:103], %[c2]\n\t" \
      "ds_read_b128 v[156:159], v122 offset:18432\n\t" \
      "ds_read_b128 v[160:163], v122 offset:18688\n\t" \
      "v_mfma_f32_16x16x32_bf16 %[c4], v[116:119], v[104:107], %[c4]\n\t" \
      "ds_read_b128 v[164:167], v122 offset:18944\n\t" \
      "ds_read_b128 v[168:171], v122 offset:19200\n\t" \
      "v_mfma_f32_16x16x32_bf16 %[c6], v[116:119], v[108:111], %[c6]\n\t" \
      "ds_read_b128 v[172:175], v122 offset:19456\n\t" \
      "v_mfma_f32_16x16x32_bf16 %[c8], v[116:119], v[112:115], %[c8]\n\t" \
      "s_waitcnt lgkmcnt(7)\n\t" \
      "v_mfma_f32_16x16x32_bf16 %[c0], v[148:151], v[128:131], %[c0]\n\t" \
      "ds_read_b64_tr_b16 v[116:117], v123 offset:27648\n\t" \
      "ds_read_b64_tr_b16 v[118:119], v123 offset:28160\n\t" \
      "v_mfma_f32_16x16x32_bf16 %[c2], v[148:151], v[132:135], %[c2]\n\t" \
      "ds_read_b128 v[96:99], v122 offset:27648\n\t" \
      "ds_read_b128 v[100:103], v122 offset:27904\n\t" \
      "v_mfma_f32_16x16x32_bf16 %[c4], v[148:151], v[136:139], %[c4]\n\t" \
      "ds_read_b128 v[104:107], v122 offset:28160\n\t" \
      "ds_read_b128 v[108:111], v122 offset:28416\n\t" \
      "v_mfma_f32_16x16x32_bf16 %[c6], v[148:151], v[140:143], %[c6]\n\t" \
      "ds_read_b128 v[112:115], v122 offset:28672\n\t" \
      "v_mfma_f32_16x16x32_bf16 %[c8], v[148:151], v[144:147], %[c8]\n\t" \
      "s_waitcnt lgkmcnt(7)\n\t" \
      "v_mfma_f32_16x16x32_bf16 %[c0], v[176:179], v[156:159], %[c0]\n\t" \
      "ds_read_b128 %[c1], %[xr] offset:0\n\t" \
      "ds_read_b128 %[c3], %[xr] offset:1024\n\t" \
      "v_mfma_f32_16x16x32_bf16 %[c2], v[176:179], v[160:163], %[c2]\n\t" \
      "ds_read_b128 %[c5], %[xr] offset:2048\n\t" \
      "ds_read_b128 %[c7], %[xr] offset:3072\n\t" \
      "v_mfma_f32_16x16x32_bf16 %[c4], v[176:179], v[164:167], %[c4]\n\t" \
      "ds_read_b128 %[c9], %[xr] offset:4096\n\t" \
      "v_mfma_f32_16x16x32_bf16 %[c6], v[176:179], v[168:171], %[c6]\n\t" \
      "v_mfma_f32_16x16x32_bf16 %[c8], v[176:179], v[172:175], %[c8]\n\t" \
      "s_waitcnt lgkmcnt(5)\n\t" \
      "s_barrier\n\t" \
      "v_mfma_f32_16x16x32_bf16 %[c0], v[116:119], v[96:99], %[c0]\n\t" \
      "v_mfma_f32_16x16x32_bf16 %[c2], v[116:119], v[100:103], %[c2]\n\t" \
      "v_mfma_f32_16x16x32_bf16 %[c4], v[116:119], v[104:107], %[c4]\n\t" \
      "v_mfma_f32_16x16x32_bf16 %[c6], v[116:119], v[108:111], %[c6]\n\t" \
      "v_mfma_f32_16x16x32_bf16 %[c8], v[116:119], v[112:115], %[c8]\n\t" \
      "s_waitcnt lgkmcnt(0)\n\t" \
      "s_branch 9f\n\t" \
      "7:\n\t" \
      "buffer_store_dword %[tag], %[fo], %[rw], 0 offen sc1\n\t" \
      "s_branch 8b\n\t" \
      "9:\n\t" \
      "s_nop 7\n\t"
#define Q_LOOP \
      "v_add_u32 v120, %[cur], %[la]\n\t" \
      "v_add_u32 v122, %[cur], %[lb]\n\t" \
      "v_xor_b32 v121, 64, v120\n\t" \
      "v_xor_b32 v123, 32, v122\n\t" \
      "v_xor_b32 v124, 64, v122\n\t" \
      "v_xor_b32 v125, 96, v122\n\t" \
      "ds_read_b64_tr_b16 v[144:145], v122 offset:0\n\t" \
      "ds_read_b64_tr_b16 v[146:147], v122 offset:1024\n\t" \
      "ds_read_b64_tr_b16 v[148:149], v123 offset:0\n\t" \
      "ds_read_b64_tr_b16 v[150:151], v123 offset:1024\n\t" \
      "ds_read_b64_tr_b16 v[152:153], v124 offset:0\n\t" \
      "ds_read_b64_tr_b16 v[154:155], v124 offset:1024\n\t" \
      "ds_read_b64_tr_b16 v[156:157], v125 offset:0\n\t" \
      "ds_read_b64_tr_b16 v[158:159], v125 offset:1024\n\t" \
      "ds_read_b128 v[128:131], v120 offset:0\n\t" \
      "ds_read_b128 v[132:135], v120 offset:512\n\t" \
      "ds_read_b128 v[136:139], v120 offset:4096\n\t" \
      "ds_read_b128 v[140:143], v120 offset:4608\n\t" \
      "s_branch 2f\n\t" \
      "1:\n\t" \
      "v_mfma_f32_16x16x32_bf16 %[c0], v[160:163], v[176:179], %[c0]\n\t" \
      "ds_read_b64_tr_b16 v[144:145], v122 offset:0\n\t" \
      "v_mfma_f32_16x16x32_bf16 %[c1], v[160:163], v[180:183], %[c1]\n\t" \
      "ds_read_b64_tr_b16 v[146:147], v122 offset:1024\n\t" \
      "v_mfma_f32_16x16x32_bf16 %[c2], v[160:163], v[184:187], %[c2]\n\t" \
      "ds_read_b64_tr_b16 v[148:149], v123 offset:0\n\t" \
      "v_mfma_f32_16x16x32_bf16 %[c3], v[160:163], v[188:191], %[c3]\n\t" \
      "ds_read_b64_tr_b16 v[150:151], v123 offset:1024\n\t" \
      "v_mfma_f32_16x16x32_bf16 %[c4], v[164:167], v[176:179], %[c4]\n\t" \
      "ds_read_b64_tr_b16 v[152:153], v124 offset:0\n\t" \
      "v_mfma_f32_16x16x32_bf16 %[c5], v[164:167], v[180:183], %[c5]\n\t" \
      "ds_read_b64_tr_b16 v[154:155], v124 offset:1024\n\t" \
      "v_mfma_f32_16x16x32_bf16 %[c6], v[164:167], v[184:187], %[c6]\n\t" \
      "ds_read_b64_tr_b16 v[156:157], v125 offset:0\n\t" \
      "v_mfma_f32_16x16x32_bf16 %[c7], v[164:167], v[188:191], %[c7]\n\t" \
      "ds_read_b64_tr_b16 v[158:159], v125 offset:1024\n\t" \
      "v_mfma_f32_16x16x32_bf16 %[c8], v[168:171], v[176:179], %[c8]\n\t" \
      "ds_read_b128 v[128:131], v120 offset:0\n\t" \
      "v_mfma_f32_16x16x32_bf16 %[c9], v[168:171], v[180:183], %[c9]\n\t" \
      "ds_read_b128 v[132:135], v120 offset:512\n\t" \
      "v_mfma_f32_16x16x32_bf16 %[c10], v[168:171], v[184:187], %[c10]\n\t" \
      "ds_read_b128 v[136:139], v120 offset:4096\n\t" \
      "v_mfma_f32_16x16x32_bf16 %[c11], v[168:171], v[188:191], %[c11]\n\t" \
      "ds_read_b128 v[140:143], v120 offset:4608\n\t" \
      "v_mfma_f32_16x16x32_bf16 %[c12], v[172:175], v[176:179], %[c12]\n\t" \
      "s_cmp_eq_u32 %[cnt], %[hk]\n\t" \
      "v_mfma_f32_16x16x32_bf16 %[c13], v[172:175], v[180:183], %[c13]\n\t" \
      "s_cbranch_scc1 7f\n\t8:\n\t" \
      "v_mfma_f32_16x16x32_bf16 %[c14], v[172:175], v[184:187], %[c14]\n\t" \
      "v_mfma_f32_16x16x32_bf16 %[c15], v[172:175], v[188:191], %[c15]\n\t" \
      "2:\n\t" \
      "s_waitcnt lgkmcnt(0)\n\t" \
      "v_mfma_f32_16x16x32_bf16 %[c0], v[128:131], v[144:147], %[c0]\n\t" \
      "ds_read_b64_tr_b16 v[176:177], v122 offset:8192\n\t" \
      "v_mfma_f32_16x16x32_bf16 %[c1], v[128:131], v[148:151], %[c1]\n\t" \
      "ds_read_b64_tr_b16 v[178:179], v122 offset:9216\n\t" \
      "v_mfma_f32_16x16x32_bf16 %[c2], v[128:131], v[152:155], %[c2]\n\t" \
      "ds_read_b64_tr_b16 v[180:181], v123 offset:8192\n\t" \
      "v_mfma_f32_16x16x32_bf16 %[c3], v[128:131], v[156:159], %[c3]\n\t" \
      "ds_read_b64_tr_b16 v[182:183], v123 offset:9216\n\t" \
      "v_mfma_f32_16x16x32_bf16 %[c4], v[132:135], v[144:147], %[c4]\n\t" \
      "ds_read_b64_tr_b16 v[184:185], v124 offset:8192\n\t" \
      "v_mfma_f32_16x16x32_bf16 %[c5], v[132:135], v[148:151], %[c5]\n\t" \
      "ds_read_b64_tr_b16 v[186:187], v124 offset:9216\n\t" \
      "v_mfma_f32_16x16x32_bf16 %[c6], v[132:135], v[152:155], %[c6]\n\t" \
      "ds_read_b64_tr_b16 v[188:189], v125 offset:8192\n\t" \
      "v_mfma_f32_16x16x32_bf16 %[c7], v[132:135], v[156:159], %[c7]\n\t" \
      "ds_read_b64_tr_b16 v[190:191], v125 offset:9216\n\t" \
      "v_mfma_f32_16x16x32_bf16 %[c8], v[136:139], v[144:147], %[c8]\n\t" \
      "ds_read_b128 v[160:163], v121 offset:0\n\t" \
      "v_mfma_f32_16x16x32_bf16 %[c9], v[136:139], v[148:151], %[c9]\n\t" \
      "ds_read_b128 v[164:167], v121 offset:512\n\t" \
      "v_mfma_f32_16x16x32_bf16 %[c10], v[136:139], v[152:155], %[c10]\n\t" \
      "ds_read_b128 v[168:171], v121 offset:4096\n\t" \
      "v_mfma_f32_16x16x32_bf16 %[c11], v[136:139], v[156:159], %[c11]\n\t" \
      "ds_read_b128 v[172:175], v121 offset:4608\n\t" \
      "v_mfma_f32_16x16x32_bf16 %[c12], v[140:143], v[144:147], %[c12]\n\t" \
      "s_add_u32 %[cur], %[cur], 32768\n\t" \
      "s_and_b32 %[cur], %[cur], 0x18000\n\t" \
      "v_add_u32 v120, %[cur], %[la]\n\t" \
      "v_mfma_f32_16x16x32_bf16 %[c13], v[140:143], v[148:151], %[c13]\n\t" \
      "v_add_u32 v122, %[cur], %[lb]\n\t" \
      "v_xor_b32 v121, 64, v120\n\t" \
      "v_mfma_f32_16x16x32_bf16 %[c14], v[140:143], v[152:155], %[c14]\n\t" \
      "v_xor_b32 v123, 32, v122\n\t" \
      "v_xor_b32 v124, 64, v122\n\t" \
      "v_mfma_f32_16x16x32_bf16 %[c15], v[140:143], v[156:159], %[c15]\n\t" \
      "v_xor_b32 v125, 96, v122\n\t" \
      "s_add_i32 %[cnt], %[cnt], -1\n\t" \
      "s_cmp_lg_u32 %[cnt], 0\n\t" \
      "s_waitcnt lgkmcnt(0)\n\t" \
      "s_barrier\n\t" \
      "s_cbranch_scc1 1b\n\t" \
      "v_mfma_f32_16x16x32_bf16 %[c0], v[160:163], v[176:179], %[c0]\n\t" \
      "ds_read_b64 v[120:121], %[co] offset:0\n\t" \
      "v_mfma_f32_16x16x32_bf16 %[c1], v[160:163], v[180:183], %[c1]\n\t" \
      "ds_read_b64 v[122:123], %[co] offset:128\n\t" \
      "v_mfma_f32_16x16x32_bf16 %[c2], v[160:163], v[184:187], %[c2]\n\t" \
      "ds_read_b64 v[124:125], %[co] offset:256\n\t" \
      "v_mfma_f32_16x16x32_bf16 %[c3], v[160:163], v[188:191], %[c3]\n\t" \
      "ds_read_b64 v[126:127], %[co] offset:384\n\t" \
      "v_mfma_f32_16x16x32_bf16 %[c4], v[164:167], v[176:179], %[c4]\n\t" \
      "ds_read_b128 v[128:131], %[so] offset:0\n\t" \
      "v_mfma_f32_16x16x32_bf16 %[c5], v[164:167], v[180:183], %[c5]\n\t" \
      "ds_read_b128 v[144:147], %[so] offset:32\n\t" \
      "v_mfma_f32_16x16x32_bf16 %[c6], v[164:167], v[184:187], %[c6]\n\t" \
      "ds_read_b128 v[132:135], %[so] offset:16\n\t" \
      "v_mfma_f32_16x16x32_bf16 %[c7], v[164:167], v[188:191], %[c7]\n\t" \
      "ds_read_b128 v[148:151], %[so] offset:48\n\t" \
      "v_mfma_f32_16x16x32_bf16 %[c8], v[168:171], v[176:179], %[c8]\n\t" \
      "ds_read_b128 v[136:139], %[so] offset:256\n\t" \
      "v_mfma_f32_16x16x32_bf16 %[c9], v[168:171], v[180:183], %[c9]\n\t" \
      "ds_read_b128 v[152:155], %[so] offset:288\n\t" \
      "v_mfma_f32_16x16x32_bf16 %[c10], v[168:171], v[184:187], %[c10]\n\t" \
      "ds_read_b128 v[140:143], %[so] offset:272\n\t" \
      "v_mfma_f32_16x16x32_bf16 %[c11], v[168:171], v[188:191], %[c11]\n\t" \
      "ds_read_b128 v[156:159], %[so] offset:304\n\t" \
      "v_mfma_f32_16x16x32_bf16 %[c12], v[172:175], v[176:179], %[c12]\n\t" \
      "v_mfma_f32_16x16x32_bf16 %[c13], v[172:175], v[180:183], %[c13]\n\t" \
      "v_mfma_f32_16x16x32_bf16 %[c14], v[172:175], v[184:187], %[c14]\n\t" \
      "v_mfma_f32_16x16x32_bf16 %[c15], v[172:175], v[188:191], %[c15]\n\t" \
      "s_waitcnt lgkmcnt(0)\n\t" \
      "s_branch 9f\n\t" \
      "7:\n\t" \
      "buffer_store_dword %[tag], %[fo], %[rw], 0 offen sc1\n\t" \
      "s_branch 8b\n\t" \
      "9:\n\t" \
      "s_nop 7\n\t"
#define H_LOOP \
      "v_add_u32 v120, %[cur], %[la]\n\t" \
      "v_add_u32 v121, %[cur], %[lb]\n\t" \
      "v_xor_b32 v122, 32, v121\n\t" \
      "v_xor_b32 v123, 64, v121\n\t" \
      "v_xor_b32 v124, 96, v121\n\t" \
      "ds_read_b64_tr_b16 v[144:145], v121 offset:0\n\t" \
      "ds_read_b64_tr_b16 v[146:147], v121 offset:1024\n\t" \
      "ds_read_b64_tr_b16 v[148:149], v122 offset:0\n\t" \
      "ds_read_b64_tr_b16 v[150:151], v122 offset:1024\n\t" \
      "ds_read_b64_tr_b16 v[152:153], v123 offset:0\n\t" \
      "ds_read_b64_tr_b16 v[154:155], v123 offset:1024\n\t" \
      "ds_read_b64_tr_b16 v[156:157], v124 offset:0\n\t" \
      "ds_read_b64_tr_b16 v[158:159], v124 offset:1024\n\t" \
      "ds_read_b128 v[128:131], v120 offset:0\n\t" \
      "ds_read_b128 v[132:135], v120 offset:256\n\t" \
      "ds_read_b128 v[136:139], v120 offset:512\n\t" \
      "ds_read_b128 v[140:143], v120 offset:768\n\t" \
      "s_branch 2f\n\t" \
      "1:\n\t" \
      "v_mfma_f32_16x16x32_bf16 %[c0], v[160:163], v[176:179], %[c0]\n\t" \
      "ds_read_b64_tr_b16 v[144:145], v121 offset:0\n\t" \
      "v_mfma_f32_16x16x32_bf16 %[c1], v[160:163], v[180:183], %[c1]\n\t" \
      "ds_read_b64_tr_b16 v[146:147], v121 offset:1024\n\t" \
      "v_mfma_f32_16x16x32_bf16 %[c2], v[160:163], v[184:187], %[c2]\n\t" \
      "ds_read_b64_tr_b16 v[148:149], v122 offset:0\n\t" \
      "v_mfma_f32_16x16x32_bf16 %[c3], v[160:163], v[188:191], %[c3]\n\t" \
      "ds_read_b64_tr_b16 v[150:151], v122 offset:1024\n\t" \
      "v_mfma_f32_16x16x32_bf16 %[c4], v[164:167], v[176:179], %[c4]\n\t" \
      "ds_read_b64_tr_b16 v[152:153], v123 offset:0\n\t" \
      "v_mfma_f32_16x16x32_bf16 %[c5], v[164:167], v[180:183], %[c5]\n\t" \
      "ds_read_b64_tr_b16 v[154:155], v123 offset:1024\n\t" \
      "v_mfma_f32_16x16x32_bf16 %[c6], v[164:167], v[184:187], %[c6]\n\t" \
      "ds_read_b64_tr_b16 v[156:157], v124 offset:0\n\t" \
      "v_mfma_f32_16x16x32_bf16 %[c7], v[164:167], v[188:191], %[c7]\n\t" \
      "ds_read_b64_tr_b16 v[158:159], v124 offset:1024\n\t" \
      "v_mfma_f32_16x16x32_bf16 %[c8], v[168:171], v[176:179], %[c8]\n\t" \
      "ds_read_b128 v[128:131], v120 offset:0\n\t" \
      "v_mfma_f32_16x16x32_bf16 %[c9], v[168:171], v[180:183], %[c9]\n\t" \
      "ds_read_b128 v[132:135], v120 offset:256\n\t" \
      "v_mfma_f32_16x16x32_bf16 %[c10], v[168:171], v[184:187], %[c10]\n\t" \
      "ds_read_b128 v[136:139], v120 offset:512\n\t" \
      "v_mfma_f32_16x16x32_bf16 %[c11], v[168:171], v[188:191], %[c11]\n\t" \
      "ds_read_b128 v[140:143], v120 offset:768\n\t" \
      "v_mfma_f32_16x16x32_bf16 %[c12], v[172:175], v[176:179], %[c12]\n\t" \
      "v_mfma_f32_16x16x32_bf16 %[c13], v[172:175], v[180:183], %[c13]\n\t" \
      "v_mfma_f32_16x16x32_bf16 %[c14], v[172:175], v[184:187], %[c14]\n\t" \
      "v_mfma_f32_16x16x32_bf16 %[c15], v[172:175], v[188:191], %[c15]\n\t" \
      "2:\n\t" \
      "s_waitcnt lgkmcnt(0)\n\t" \
      "v_mfma_f32_16x16x32_bf16 %[c0], v[128:131], v[144:147], %[c0]\n\t" \
      "ds_read_b64_tr_b16 v[176:177], v121 offset:8192\n\t" \
      "v_mfma_f32_16x16x32_bf16 %[c1], v[128:131], v[148:151], %[c1]\n\t" \
      "ds_read_b64_tr_b16 v[178:179], v121 offset:9216\n\t" \
      "v_mfma_f32_16x16x32_bf16 %[c2], v[128:131], v[152:155], %[c2]\n\t" \
      "ds_read_b64_tr_b16 v[180:181], v122 offset:8192\n\t" \
      "v_mfma_f32_16x16x32_bf16 %[c3], v[128:131], v[156:159], %[c3]\n\t" \
      "ds_read_b64_tr_b16 v[182:183], v122 offset:9216\n\t" \
      "v_mfma_f32_16x16x32_bf16 %[c4], v[132:135], v[144:147], %[c4]\n\t" \
      "ds_read_b64_tr_b16 v[184:185], v123 offset:8192\n\t" \
      "v_mfma_f32_16x16x32_bf16 %[c5], v[132:135], v[148:151], %[c5]\n\t" \
      "ds_read_b64_tr_b16 v[186:187], v123 offset:9216\n\t" \
      "v_mfma_f32_16x16x32_bf16 %[c6], v[132:135], v[152:155], %[c6]\n\t" \
      "ds_read_b64_tr_b16 v[188:189], v124 offset:8192\n\t" \
      "v_mfma_f32_16x16x32_bf16 %[c7], v[132:135], v[156:159], %[c7]\n\t" \
      "ds_read_b64_tr_b16 v[190:191], v124 offset:9216\n\t" \
      "v_mfma_f32_16x16x32_bf16 %[c8], v[136:139], v[144:147], %[c8]\n\t" \
      "ds_read_b128 v[160:163], v120 offset:4096\n\t" \
      "v_mfma_f32_16x16x32_bf16 %[c9], v[136:139], v[148:151], %[c9]\n\t" \
      "ds_read_b128 v[164:167], v120 offset:4352\n\t" \
      "v_mfma_f32_16x16x32_bf16 %[c10], v[136:139], v[152:155], %[c10]\n\t" \
      "ds_read_b128 v[168:171], v120 offset:4608\n\t" \
      "v_mfma_f32_16x16x32_bf16 %[c11], v[136:139], v[156:159], %[c11]\n\t" \
      "ds_read_b128 v[172:175], v120 offset:4864\n\t" \
      "v_mfma_f32_16x16x32_bf16 %[c12], v[140:143], v[144:147], %[c12]\n\t" \
      "s_add_u32 %[cur], %[cur], 32768\n\t" \
      "s_and_b32 %[cur], %[cur], 0x18000\n\t" \
      "v_mfma_f32_16x16x32_bf16 %[c13], v[140:143], v[148:151], %[c13]\n\t" \
      "v_add_u32 v120, %[cur], %[la]\n\t" \
      "v_add_u32 v121, %[cur], %[lb]\n\t" \
      "v_mfma_f32_16x16x32_bf16 %[c14], v[140:143], v[152:155], %[c14]\n\t" \
      "v_xor_b32 v122, 32, v121\n\t" \
      "v_xor_b32 v123, 64, v121\n\t" \
      "v_mfma_f32_16x16x32_bf16 %[c15], v[140:143], v[156:159], %[c15]\n\t" \
      "v_xor_b32 v124, 96, v121\n\t" \
      "s_add_i32 %[cnt], %[cnt], -1\n\t" \
      "s_cmp_lg_u32 %[cnt], 0\n\t" \
      "s_waitcnt lgkmcnt(0)\n\t" \
      "s_barrier\n\t" \
      "s_cbranch_scc1 1b\n\t" \
      "v_mfma_f32_16x16x32_bf16 %[c0], v[160:163], v[176:179], %[c0]\n\t" \
      "v_mfma_f32_16x16x32_bf16 %[c1], v[160:163], v[180:183], %[c1]\n\t" \
      "v_mfma_f32_16x16x32_bf16 %[c2], v[160:163], v[184:187], %[c2]\n\t" \
      "v_mfma_f32_16x16x32_bf16 %[c3], v[160:163], v[188:191], %[c3]\n\t" \
      "v_mfma_f32_16x16x32_bf16 %[c4], v[164:167], v[176:179], %[c4]\n\t" \
      "v_mfma_f32_16x16x32_bf16 %[c5], v[164:167], v[180:183], %[c5]\n\t" \
      "v_mfma_f32_16x16x32_bf16 %[c6], v[164:167], v[184:187], %[c6]\n\t" \
      "v_mfma_f32_16x16x32_bf16 %[c7], v[164:167], v[188:191], %[c7]\n\t" \
      "v_mfma_f32_16x16x32_bf16 %[c8], v[168:171], v[176:179], %[c8]\n\t" \
      "v_mfma_f32_16x16x32_bf16 %[c9], v[168:171], v[180:183], %[c9]\n\t" \
      "v_mfma_f32_16x16x32_bf16 %[c10], v[168:171], v[184:187], %[c10]\n\t" \
      "v_mfma_f32_16x16x32_bf16 %[c11], v[168:171], v[188:191], %[c11]\n\t" \
      "v_mfma_f32_16x16x32_bf16 %[c12], v[172:175], v[176:179], %[c12]\n\t" \
      "v_mfma_f32_16x16x32_bf16 %[c13], v[172:175], v[180:183], %[c13]\n\t" \
      "v_mfma_f32_16x16x32_bf16 %[c14], v[172:175], v[184:187], %[c14]\n\t" \
      "v_mfma_f32_16x16x32_bf16 %[c15], v[172:175], v[188:191], %[c15]\n\t" \
      "s_nop 7\n\t"
#define Y_LOOP \
      "v_add_u32 v88, %[cur], %[la]\n\t" \
      "v_add_u32 v89, %[cur], %[lb]\n\t" \
      "v_xor_b32 v90, 32, v89\n\t" \
      "ds_read_b64_tr_b16 v[112:113], v89 offset:0\n\t" \
      "ds_read_b64_tr_b16 v[114:115], v89 offset:1024\n\t" \
      "ds_read_b64_tr_b16 v[116:117], v90 offset:0\n\t" \
      "ds_read_b64_tr_b16 v[118:119], v90 offset:1024\n\t" \
      "ds_read_b128 v[96:99], v88 offset:0\n\t" \
      "ds_read_b128 v[100:103], v88 offset:256\n\t" \
      "ds_read_b128 v[104:107], v88 offset:512\n\t" \
      "ds_read_b128 v[108:111], v88 offset:768\n\t" \
      "ds_read_b64_tr_b16 v[136:137], v89 offset:8192\n\t" \
      "ds_read_b64_tr_b16 v[138:139], v89 offset:9216\n\t" \
      "ds_read_b64_tr_b16 v[140:141], v90 offset:8192\n\t" \
      "ds_read_b64_tr_b16 v[142:143], v90 offset:9216\n\t" \
      "ds_read_b128 v[120:123], v88 offset:4096\n\t" \
      "ds_read_b128 v[124:127], v88 offset:4352\n\t" \
      "ds_read_b128 v[128:131], v88 offset:4608\n\t" \
      "ds_read_b128 v[132:135], v88 offset:4864\n\t" \
      "s_add_u32 %[cur], %[cur], 24576\n\t" \
      "s_cmp_eq_u32 %[cur], 147456\n\t" \
      "s_cselect_b32 %[cur], 0, %[cur]\n\t" \
      "v_add_u32 v88, %[cur], %[la]\n\t" \
      "v_add_u32 v89, %[cur], %[lb]\n\t" \
      "v_xor_b32 v90, 32, v89\n\t" \
      "1:\n\t" \
      "s_waitcnt lgkmcnt(8)\n\t" \
      "s_barrier\n\t" \
      "v_mfma_f32_16x16x32_bf16 %[c0], v[96:99], v[112:115], %[c0]\n\t" \
      "ds_read_b64_tr_b16 v[160:161], v89 offset:0\n\t" \
      "v_mfma_f32_16x16x32_bf16 %[c1], v[96:99], v[116:119], %[c1]\n\t" \
      "ds_read_b64_tr_b16 v[162:163], v89 offset:1024\n\t" \
      "v_mfma_f32_16x16x32_bf16 %[c2], v[100:103], v[112:115], %[c2]\n\t" \
      "ds_read_b64_tr_b16 v[164:165], v90 offset:0\n\t" \
      "v_mfma_f32_16x16x32_bf16 %[c3], v[100:103], v[116:119], %[c3]\n\t" \
      "ds_read_b64_tr_b16 v[166:167], v90 offset:1024\n\t" \
      "v_mfma_f32_16x16x32_bf16 %[c4], v[104:107], v[112:115], %[c4]\n\t" \
      "ds_read_b128 v[144:147], v88 offset:0\n\t" \
      "s_add_u32 %[cur], %[cur], 24576\n\t" \
      "v_mfma_f32_16x16x32_bf16 %[c5], v[104:107], v[116:119], %[c5]\n\t" \
      "ds_read_b128 v[148:151], v88 offset:256\n\t" \
      "s_cmp_eq_u32 %[cur], 147456\n\t" \
      "v_mfma_f32_16x16x32_bf16 %[c6], v[108:111], v[112:115], %[c6]\n\t" \
      "ds_read_b128 v[152:155], v88 offset:512\n\t" \
      "s_cselect_b32 %[cur], 0, %[cur]\n\t" \
      "v_mfma_f32_16x16x32_bf16 %[c7], v[108:111], v[116:119], %[c7]\n\t" \
      "ds_read_b128 v[156:159], v88 offset:768\n\t" \
      "s_waitcnt lgkmcnt(8)\n\t" \
      "v_mfma_f32_16x16x32_bf16 %[c0], v[120:123], v[136:139], %[c0]\n\t" \
      "ds_read_b64_tr_b16 v[184:185], v89 offset:8192\n\t" \
      "v_mfma_f32_16x16x32_bf16 %[c1], v[120:123], v[140:143], %[c1]\n\t" \
      "ds_read_b64_tr_b16 v[186:187], v89 offset:9216\n\t" \
      "v_mfma_f32_16x16x32_bf16 %[c2], v[124:127], v[136:139], %[c2]\n\t" \
      "ds_read_b64_tr_b16 v[188:189], v90 offset:8192\n\t" \
      "v_mfma_f32_16x16x32_bf16 %[c3], v[124:127], v[140:143], %[c3]\n\t" \
      "ds_read_b64_tr_b16 v[190:191], v90 offset:9216\n\t" \
      "v_mfma_f32_16x16x32_bf16 %[c4], v[128:131], v[136:139], %[c4]\n\t" \
      "ds_read_b128 v[168:171], v88 offset:4096\n\t" \
      "v_mfma_f32_16x16x32_bf16 %[c5], v[128:131], v[140:143], %[c5]\n\t" \
      "ds_read_b128 v[172:175], v88 offset:4352\n\t" \
      "v_mfma_f32_16x16x32_bf16 %[c6], v[132:135], v[136:139], %[c6]\n\t" \
      "ds_read_b128 v[176:179], v88 offset:4608\n\t" \
      "v_mfma_f32_16x16x32_bf16 %[c7], v[132:135], v[140:143], %[c7]\n\t" \
      "ds_read_b128 v[180:183], v88 offset:4864\n\t" \
      "v_add_u32 v88, %[cur], %[la]\n\t" \
      "v_add_u32 v89, %[cur], %[lb]\n\t" \
      "v_xor_b32 v90, 32, v89\n\t" \
      "s_waitcnt lgkmcnt(8)\n\t" \
      "s_barrier\n\t" \
      "v_mfma_f32_16x16x32_bf16 %[c0], v[144:147], v[160:163], %[c0]\n\t" \
      "ds_read_b64_tr_b16 v[112:113], v89 offset:0\n\t" \
      "v_mfma_f32_16x16x32_bf16 %[c1], v[144:147], v[164:167], %[c1]\n\t" \
      "ds_read_b64_tr_b16 v[114:115], v89 offset:1024\n\t" \
      "v_mfma_f32_16x16x32_bf16 %[c2], v[148:151], v[160:163], %[c2]\n\t" \
      "ds_read_b64_tr_b16 v[116:117], v90 offset:0\n\t" \
      "v_mfma_f32_16x16x32_bf16 %[c3], v[148:151], v[164:167], %[c3]\n\t" \
      "ds_read_b64_tr_b16 v[118:119], v90 offset:1024\n\t" \
      "v_mfma_f32_16x16x32_bf16 %[c4], v[152:155], v[160:163], %[c4]\n\t" \
      "ds_read_b128 v[96:99], v88 offset:0\n\t" \
      "s_add_u32 %[cur], %[cur], 24576\n\t" \
      "v_mfma_f32_16x16x32_bf16 %[c5], v[152:155], v[164:167], %[c5]\n\t" \
      "ds_read_b128 v[100:103], v88 offset:256\n\t" \
      "s_cmp_eq_u32 %[cur], 147456\n\t" \
      "v_mfma_f32_16x16x32_bf16 %[c6], v[156:159], v[160:163], %[c6]\n\t" \
      "ds_read_b128 v[104:107], v88 offset:512\n\t" \
      "s_cselect_b32 %[cur], 0, %[cur]\n\t" \
      "v_mfma_f32_16x16x32_bf16 %[c7], v[156:159], v[164:167], %[c7]\n\t" \
      "ds_read_b128 v[108:111], v88 offset:768\n\t" \
      "s_add_i32 %[cnt], %[cnt], -1\n\t" \
      "s_waitcnt lgkmcnt(8)\n\t" \
      "v_mfma_f32_16x16x32_bf16 %[c0], v[168:171], v[184:187], %[c0]\n\t" \
      "ds_read_b64_tr_b16 v[136:137], v89 offset:8192\n\t" \
      "v_mfma_f32_16x16x32_bf16 %[c1], v[168:171], v[188:191], %[c1]\n\t" \
      "ds_read_b64_tr_b16 v[138:139], v89 offset:9216\n\t" \
      "v_mfma_f32_16x16x32_bf16 %[c2], v[172:175], v[184:187], %[c2]\n\t" \
      "ds_read_b64_tr_b16 v[140:141], v90 offset:8192\n\t" \
      "v_mfma_f32_16x16x32_bf16 %[c3], v[172:175], v[188:191], %[c3]\n\t" \
      "ds_read_b64_tr_b16 v[142:143], v90 offset:9216\n\t" \
      "v_mfma_f32_16x16x32_bf16 %[c4], v[176:179], v[184:187], %[c4]\n\t" \
      "ds_read_b128 v[120:123], v88 offset:4096\n\t" \
      "s_cmp_lg_u32 %[cnt], 0\n\t" \
      "v_mfma_f32_16x16x32_bf16 %[c5], v[176:179], v[188:191], %[c5]\n\t" \
      "ds_read_b128 v[124:127], v88 offset:4352\n\t" \
      "v_mfma_f32_16x16x32_bf16 %[c6], v[180:183], v[184:187], %[c6]\n\t" \
      "ds_read_b128 v[128:131], v88 offset:4608\n\t" \
      "v_mfma_f32_16x16x32_bf16 %[c7], v[180:183], v[188:191], %[c7]\n\t" \
      "ds_read_b128 v[132:135], v88 offset:4864\n\t" \
      "v_add_u32 v88, %[cur], %[la]\n\t" \
      "v_add_u32 v89, %[cur], %[lb]\n\t" \
      "v_xor_b32 v90, 32, v89\n\t" \
      "s_cbranch_scc1 1b\n\t" \
      "v_add_u32 v88, %[cur], %[lc]\n\t" \
      "v_add_u32 v92, %[cur], %[ld]\n\t" \
      "v_xor_b32 v89, 32, v88\n\t" \
      "v_xor_b32 v90, 64, v88\n\t" \
      "v_xor_b32 v91, 96, v88\n\t" \
      "v_xor_b32 v93, 0x80, v92\n\t" \
      "s_mov_b32 %[cnt], 5\n\t" \
      "2:\n\t" \
      "s_waitcnt lgkmcnt(8)\n\t" \
      "s_barrier\n\t" \
      "v_mfma_f32_16x16x32_bf16 %[c0], v[96:99], v[112:115], %[c0]\n\t" \
      "ds_read_b64 v[160:161], v92 offset:0\n\t" \
      "ds_read_b64 v[162:163], v92 offset:1024\n\t" \
      "v_mfma_f32_16x16x32_bf16 %[c1], v[96:99], v[116:119], %[c1]\n\t" \
      "ds_read_b64 v[164:165], v93 offset:0\n\t" \
      "ds_read_b64 v[166:167], v93 offset:1024\n\t" \
      "v_mfma_f32_16x16x32_bf16 %[c2], v[100:103], v[112:115], %[c2]\n\t" \
      "ds_read_b64_tr_b16 v[144:145], v88 offset:0\n\t" \
      "ds_read_b64_tr_b16 v[146:147], v88 offset:512\n\t" \
      "v_mfma_f32_16x16x32_bf16 %[c3], v[100:103], v[116:119], %[c3]\n\t" \
      "ds_read_b64_tr_b16 v[148:149], v89 offset:0\n\t" \
      "ds_read_b64_tr_b16 v[150:151], v89 offset:512\n\t" \
      "v_mfma_f32_16x16x32_bf16 %[c4], v[104:107], v[112:115], %[c4]\n\t" \
      "ds_read_b64_tr_b16 v[152:153], v90 offset:0\n\t" \
      "s_add_u32 %[cur], %[cur], 24576\n\t" \
      "v_mfma_f32_16x16x32_bf16 %[c5], v[104:107], v[116:119], %[c5]\n\t" \
      "ds_read_b64_tr_b16 v[154:155], v90 offset:512\n\t" \
      "s_cmp_eq_u32 %[cur], 147456\n\t" \
      "v_mfma_f32_16x16x32_bf16 %[c6], v[108:111], v[112:115], %[c6]\n\t" \
      "ds_read_b64_tr_b16 v[156:157], v91 offset:0\n\t" \
      "s_cselect_b32 %[cur], 0, %[cur]\n\t" \
      "v_mfma_f32_16x16x32_bf16 %[c7], v[108:111], v[116:119], %[c7]\n\t" \
      "ds_read_b64_tr_b16 v[158:159], v91 offset:512\n\t" \
      "s_waitcnt lgkmcnt(12)\n\t" \
      "v_mfma_f32_16x16x32_bf16 %[c0], v[120:123], v[136:139], %[c0]\n\t" \
      "ds_read_b64 v[184:185], v92 offset:8192\n\t" \
      "ds_read_b64 v[186:187], v92 offset:9216\n\t" \
      "v_mfma_f32_16x16x32_bf16 %[c1], v[120:123], v[140:143], %[c1]\n\t" \
      "ds_read_b64 v[188:189], v93 offset:8192\n\t" \
      "ds_read_b64 v[190:191], v93 offset:9216\n\t" \
      "v_mfma_f32_16x16x32_bf16 %[c2], v[124:127], v[136:139], %[c2]\n\t" \
      "ds_read_b64_tr_b16 v[168:169], v88 offset:4096\n\t" \
      "ds_read_b64_tr_b16 v[170:171], v88 offset:4608\n\t" \
      "v_mfma_f32_16x16x32_bf16 %[c3], v[124:127], v[140:143], %[c3]\n\t" \
      "ds_read_b64_tr_b16 v[172:173], v89 offset:4096\n\t" \
      "ds_read_b64_tr_b16 v[174:175], v89 offset:4608\n\t" \
      "v_mfma_f32_16x16x32_bf16 %[c4], v[128:131], v[136:139], %[c4]\n\t" \
      "ds_read_b64_tr_b16 v[176:177], v90 offset:4096\n\t" \
      "v_mfma_f32_16x16x32_bf16 %[c5], v[128:131], v[140:143], %[c5]\n\t" \
      "ds_read_b64_tr_b16 v[178:179], v90 offset:4608\n\t" \
      "v_mfma_f32_16x16x32_bf16 %[c6], v[132:135], v[136:139], %[c6]\n\t" \
      "ds_read_b64_tr_b16 v[180:181], v91 offset:4096\n\t" \
      "v_mfma_f32_16x16x32_bf16 %[c7], v[132:135], v[140:143], %[c7]\n\t" \
      "ds_read_b64_tr_b16 v[182:183], v91 offset:4608\n\t" \
      "v_add_u32 v88, %[cur], %[lc]\n\t" \
      "v_add_u32 v92, %[cur], %[ld]\n\t" \
      "v_xor_b32 v89, 32, v88\n\t" \
      "v_xor_b32 v90, 64, v88\n\t" \
      "v_xor_b32 v91, 96, v88\n\t" \
      "v_xor_b32 v93, 0x80, v92\n\t" \
      "s_waitcnt lgkmcnt(12)\n\t" \
      "s_barrier\n\t" \
      "v_mfma_f32_16x16x32_bf16 %[c0], v[144:147], v[160:163], %[c0]\n\t" \
      "ds_read_b64 v[112:113], v92 offset:0\n\t" \
      "ds_read_b64 v[114:115], v92 offset:1024\n\t" \
      "v_mfma_f32_16x16x32_bf16 %[c1], v[144:147], v[164:167], %[c1]\n\t" \
      "ds_read_b64 v[116:117], v93 offset:0\n\t" \
      "ds_read_b64 v[118:119], v93 offset:1024\n\t" \
      "v_mfma_f32_16x16x32_bf16 %[c2], v[148:151], v[160:163], %[c2]\n\t" \
      "ds_read_b64_tr_b16 v[96:97], v88 offset:0\n\t" \
      "ds_read_b64_tr_b16 v[98:99], v88 offset:512\n\t" \
      "v_mfma_f32_16x16x32_bf16 %[c3], v[148:151], v[164:167], %[c3]\n\t" \
      "ds_read_b64_tr_b16 v[100:101], v89 offset:0\n\t" \
      "ds_read_b64_tr_b16 v[102:103], v89 offset:512\n\t" \
      "v_mfma_f32_16x16x32_bf16 %[c4], v[152:155], v[160:163], %[c4]\n\t" \
      "ds_read_b64_tr_b16 v[104:105], v90 offset:0\n\t" \
      "s_add_u32 %[cur], %[cur], 24576\n\t" \
      "v_mfma_f32_16x16x32_bf16 %[c5], v[152:155], v[164:167], %[c5]\n\t" \
      "ds_read_b64_tr_b16 v[106:107], v90 offset:512\n\t" \
      "s_cmp_eq_u32 %[cur], 147456\n\t" \
      "v_mfma_f32_16x16x32_bf16 %[c6], v[156:159], v[160:163], %[c6]\n\t" \
      "ds_read_b64_tr_b16 v[108:109], v91 offset:0\n\t" \
      "s_cselect_b32 %[cur], 0, %[cur]\n\t" \
      "v_mfma_f32_16x16x32_bf16 %[c7], v[156:159], v[164:167], %[c7]\n\t" \
      "ds_read_b64_tr_b16 v[110:111], v91 offset:512\n\t" \
      "s_add_i32 %[cnt], %[cnt], -1\n\t" \
      "s_waitcnt lgkmcnt(12)\n\t" \
      "v_mfma_f32_16x16x32_bf16 %[c0], v[168:171], v[184:187], %[c0]\n\t" \
      "ds_read_b64 v[136:137], v92 offset:8192\n\t" \
      "ds_read_b64 v[138:139], v92 offset:9216\n\t" \
      "v_mfma_f32_16x16x32_bf16 %[c1], v[168:171], v[188:191], %[c1]\n\t" \
      "ds_read_b64 v[140:141], v93 offset:8192\n\t" \
      "ds_read_b64 v[142:143], v93 offset:9216\n\t" \
      "v_mfma_f32_16x16x32_bf16 %[c2], v[172:175], v[184:187], %[c2]\n\t" \
      "ds_read_b64_tr_b16 v[120:121], v88 offset:4096\n\t" \
      "ds_read_b64_tr_b16 v[122:123], v88 offset:4608\n\t" \
      "v_mfma_f32_16x16x32_bf16 %[c3], v[172:175], v[188:191], %[c3]\n\t" \
      "ds_read_b64_tr_b16 v[124:125], v89 offset:4096\n\t" \
      "ds_read_b64_tr_b16 v[126:127], v89 offset:4608\n\t" \
      "v_mfma_f32_16x16x32_bf16 %[c4], v[176:179], v[184:187], %[c4]\n\t" \
      "ds_read_b64_tr_b16 v[128:129], v90 offset:4096\n\t" \
      "s_cmp_lg_u32 %[cnt], 0\n\t" \
      "v_mfma_f32_16x16x32_bf16 %[c5], v[176:179], v[188:191], %[c5]\n\t" \
      "ds_read_b64_tr_b16 v[130:131], v90 offset:4608\n\t" \
      "v_mfma_f32_16x16x32_bf16 %[c6], v[180:183], v[184:187], %[c6]\n\t" \
      "ds_read_b64_tr_b16 v[132:133], v91 offset:4096\n\t" \
      "v_mfma_f32_16x16x32_bf16 %[c7], v[180:183], v[188:191], %[c7]\n\t" \
      "ds_read_b64_tr_b16 v[134:135], v91 offset:4608\n\t" \
      "v_add_u32 v88, %[cur], %[lc]\n\t" \
      "v_add_u32 v92, %[cur], %[ld]\n\t" \
      "v_xor_b32 v89, 32, v88\n\t" \
      "v_xor_b32 v90, 64, v88\n\t" \
      "v_xor_b32 v91, 96, v88\n\t" \
      "v_xor_b32 v93, 0x80, v92\n\t" \
      "s_cbranch_scc1 2b\n\t" \
      "s_waitcnt lgkmcnt(12)\n\t" \
      "v_mfma_f32_16x16x32_bf16 %[c0], v[96:99], v[112:115], %[c0]\n\t" \
      "v_mfma_f32_16x16x32_bf16 %[c1], v[96:99], v[116:119], %[c1]\n\t" \
      "v_mfma_f32_16x16x32_bf16 %[c2], v[100:103], v[112:115], %[c2]\n\t" \
      "v_mfma_f32_16x16x32_bf16 %[c3], v[100:103], v[116:119], %[c3]\n\t" \
      "v_mfma_f32_16x16x32_bf16 %[c4], v[104:107], v[112:115], %[c4]\n\t" \
      "v_mfma_f32_16x16x32_bf16 %[c5], v[104:107], v[116:119], %[c5]\n\t" \
      "v_mfma_f32_16x16x32_bf16 %[c6], v[108:111], v[112:115], %[c6]\n\t" \
      "v_mfma_f32_16x16x32_bf16 %[c7], v[108:111], v[116:119], %[c7]\n\t" \
      "s_waitcnt lgkmcnt(0)\n\t" \
      "s_barrier\n\t" \
      "v_mfma_f32_16x16x32_bf16 %[c0], v[120:123], v[136:139], %[c0]\n\t" \
      "ds_read_b64 v[144:145], %[aa] offset:0\n\t" \
      "v_mfma_f32_16x16x32_bf16 %[c1], v[120:123], v[140:143], %[c1]\n\t" \
      "ds_read_b64 v[146:147], %[aa] offset:256\n\t" \
      "v_mfma_f32_16x16x32_bf16 %[c2], v[124:127], v[136:139], %[c2]\n\t" \
      "ds_read_b64 v[148:149], %[aa] offset:4096\n\t" \
      "v_mfma_f32_16x16x32_bf16 %[c3], v[124:127], v[140:143], %[c3]\n\t" \
      "ds_read_b64 v[150:151], %[aa] offset:4352\n\t" \
      "v_mfma_f32_16x16x32_bf16 %[c4], v[128:131], v[136:139], %[c4]\n\t" \
      "ds_read_b64 v[152:153], %[aa] offset:8192\n\t" \
      "v_mfma_f32_16x16x32_bf16 %[c5], v[128:131], v[140:143], %[c5]\n\t" \
      "ds_read_b64 v[154:155], %[aa] offset:8448\n\t" \
      "v_mfma_f32_16x16x32_bf16 %[c6], v[132:135], v[136:139], %[c6]\n\t" \
      "ds_read_b64 v[156:157], %[aa] offset:12288\n\t" \
      "v_mfma_f32_16x16x32_bf16 %[c7], v[132:135], v[140:143], %[c7]\n\t" \
      "ds_read_b64 v[158:159], %[aa] offset:12544\n\t" \
      "s_waitcnt lgkmcnt(0)\n\t" \
      "s_nop 7\n\t"
#define Y_CLOB "v88", "v89", "v90", "v91", "v92", "v93", "v94", "v95", "v96", "v97", "v98", "v99", "v100", "v101", "v102", "v103", "v104", "v105", "v106", "v107", "v108", "v109", "v110", "v111", "v112", "v113", "v114", "v115", "v116", "v117", "v118", "v119", "v120", "v121", "v122", "v123", "v124", "v125", "v126", "v127", "v128", "v129", "v130", "v131", "v132", "v133", "v134", "v135", "v136", "v137", "v138", "v139", "v140", "v141", "v142", "v143", "v160", "v161", "v162", "v163", "v164", "v165", "v166", "v167", "v168", "v169", "v170", "v171", "v172", "v173", "v174", "v175", "v176", "v177", "v178", "v179", "v180", "v181", "v182", "v183", "v184", "v185", "v186", "v187", "v188", "v189", "v190", "v191"
#define U_CLOB "v96", "v97", "v98", "v99", "v100", "v101", "v102", "v103", "v104", "v105", "v106", "v107", "v108", "v109", "v110", "v111", "v112", "v113", "v114", "v115", "v116", "v117", "v118", "v119", "v120", "v121", "v122", "v123", "v124", "v125", "v126", "v127", "v128", "v129", "v130", "v131", "v132", "v133", "v134", "v135", "v136", "v137", "v138", "v139", "v140", "v141", "v142", "v143", "v144", "v145", "v146", "v147", "v148", "v149", "v150", "v151", "v152", "v153", "v154", "v155", "v156", "v157", "v158", "v159", "v160", "v161", "v162", "v163", "v164", "v165", "v166", "v167", "v168", "v169", "v170", "v171", "v172", "v173", "v174", "v175", "v176", "v177", "v178", "v179", "v180", "v181", "v182", "v183", "v184", "v185", "v186", "v187", "v188", "v189", "v190", "v191"
#define L_CLOB "v120", "v121", "v122", "v123", "v124", "v125", "v126", "v127", "v128", "v129", "v130", "v131", "v132", "v133", "v134", "v135", "v136", "v137", "v138", "v139", "v140", "v141", "v142", "v143", "v144", "v145", "v146", "v147", "v148", "v149", "v150", "v151", "v152", "v153", "v154", "v155", "v156", "v157", "v158", "v159", "v160", "v161", "v162", "v163", "v164", "v165", "v166", "v167", "v168", "v169", "v170", "v171", "v172", "v173", "v174", "v175", "v176", "v177", "v178", "v179", "v180", "v181", "v182", "v183", "v184", "v185", "v186", "v187", "v188", "v189", "v190", "v191"
#define Q_CLOB "v160", "v161", "v162", "v163", "v164", "v165", "v166", "v167", "v168", "v169", "v170", "v171", "v172", "v173", "v174", "v175", "v176", "v177", "v178", "v179", "v180", "v181", "v182", "v183", "v184", "v185", "v186", "v187", "v188", "v189", "v190", "v191"
#define U1_ASM \
      "s_mov_b32 s62, 0\n\t" \
      "buffer_load_dword %[fv], %[f1], %[ws], 0 offen sc1\n\t" \
      "s_mov_b32 s70, 0xa311020\n\t" \
      "s_mov_b32 s71, 0x14c49cc8\n\t" \
      "s_mov_b32 s72, 0x7b9ab\n\t" \
      "s_cmp_eq_u32 %[cu], 1\n\t" \
      "s_cselect_b32 s70, 0x8328820, s70\n\t" \
      "s_cselect_b32 s71, 0x1aa41d26, s71\n\t" \
      "s_cselect_b32 s72, 0x7b98b, s72\n\t" \
      "s_cmp_eq_u32 %[cu], 2\n\t" \
      "s_cselect_b32 s70, 0x8618402, s70\n\t" \
      "s_cselect_b32 s71, 0x169428e5, s71\n\t" \
      "s_cselect_b32 s72, 0x7b58e, s72\n\t" \
      "s_cmp_eq_u32 %[cu], 3\n\t" \
      "s_cselect_b32 s70, 0xe410460, s70\n\t" \
      "s_cselect_b32 s71, 0x1495a0c5, s71\n\t" \
      "s_cselect_b32 s72, 0x735ec, s72\n\t" \
      "s_waitcnt vmcnt(17)\n\t" \
      "s_barrier\n\t" \
      "s_add_u32 m0, %[j9], 0\n\t" \
      "s_bfe_u32 s60, s70, 0x50000\n\t" \
      "s_mul_i32 s60, s60, 442368\n\t" \
      "buffer_load_dwordx4 %[b0], %[w1], s60 offen offset:0 sc1 lds\n\t" \
      "buffer_load_dwordx4 %[b1], %[w1], s60 offen offset:1024 sc1 lds\n\t" \
      "buffer_load_dwordx4 %[b2], %[w1], s60 offen offset:2048 sc1 lds\n\t" \
      "buffer_load_dwordx4 %[b3], %[w1], s60 offen offset:3072 sc1 lds\n\t" \
      "s_add_u32 s66, %[sp], 1327104\n\t" \
      "s_buffer_load_dword s63, %[w1], s66\n\t" \
      "s_buffer_load_dword s63, %[w1], s66 offset:64\n\t" \
      "s_add_u32 s66, %[sp], 1437696\n\t" \
      "s_buffer_load_dword s63, %[w1], s66\n\t" \
      "s_buffer_load_dword s63, %[w1], s66 offset:64\n\t" \
      "s_add_u32 s66, %[sp], 1548288\n\t" \
      "s_buffer_load_dword s63, %[w1], s66\n\t" \
      "s_buffer_load_dword s63, %[w1], s66 offset:64\n\t" \
      "s_add_u32 s66, %[sp], 1658880\n\t" \
      "s_buffer_load_dword s63, %[w1], s66\n\t" \
      "s_buffer_load_dword s63, %[w1], s66 offset:64\n\t" \
      "s_add_u32 s66, %[sp], 1769472\n\t" \
      "s_buffer_load_dword s63, %[w1], s66\n\t" \
      "s_buffer_load_dword s63, %[w1], s66 offset:64\n\t" \
      "s_waitcnt vmcnt(13)\n\t" \
      "s_barrier\n\t" \
      "s_add_u32 m0, %[j9], 36864\n\t" \
      "s_bfe_u32 s60, s70, 0x50005\n\t" \
      "s_mul_i32 s60, s60, 442368\n\t" \
      "buffer_load_dwordx4 %[b0], %[w1], s60 offen offset:0 sc1 lds\n\t" \
      "buffer_load_dwordx4 %[b1], %[w1], s60 offen offset:1024 sc1 lds\n\t" \
      "buffer_load_dwordx4 %[b2], %[w1], s60 offen offset:2048 sc1 lds\n\t" \
      "buffer_load_dwordx4 %[b3], %[w1], s60 offen offset:3072 sc1 lds\n\t" \
      "s_add_u32 s66, %[sp], 1880064\n\t" \
      "s_buffer_load_dword s63, %[w1], s66\n\t" \
      "s_buffer_load_dword s63, %[w1], s66 offset:64\n\t" \
      "s_add_u32 s66, %[sp], 1990656\n\t" \
      "s_buffer_load_dword s63, %[w1], s66\n\t" \
      "s_buffer_load_dword s63, %[w1], s66 offset:64\n\t" \
      "s_add_u32 s66, %[sp], 2101248\n\t" \
      "s_buffer_load_dword s63, %[w1], s66\n\t" \
      "s_buffer_load_dword s63, %[w1], s66 offset:64\n\t" \
      "s_add_u32 s66, %[sp], 2211840\n\t" \
      "s_buffer_load_dword s63, %[w1], s66\n\t" \
      "s_buffer_load_dword s63, %[w1], s66 offset:64\n\t" \
      "s_waitcnt vmcnt(8)\n\t" \
      "10:\n\t" \
      "v_cmp_ne_u32 vcc, %[tag], %[fv]\n\t" \
      "s_add_u32 s62, s62, 1\n\t" \
      "s_cbranch_vccz 110f\n\t" \
      "s_cmp_gt_u32 s62, 0x20000\n\t" \
      "s_cbranch_scc1 110f\n\t" \
      "s_sleep 2\n\t" \
      "11:\n\t" \
      "buffer_load_dword %[fv], %[f1], %[ws], 0 offen sc1\n\t" \
      "s_waitcnt vmcnt(0)\n\t" \
      "s_branch 10b\n\t" \
      "110:\n\t" \
      "s_barrier\n\t" \
      "s_add_u32 m0, %[sb], 0\n\t" \
      "s_nop 0\n\t" \
      "buffer_load_dwordx4 %[st], %[ws], 0 offen offset:0 sc1 lds\n\t" \
      "s_add_u32 m0, %[j9], 4096\n\t" \
      "s_bfe_u32 s61, s70, 0x50000\n\t" \
      "s_mul_i32 s61, s61, 20480\n\t" \
      "buffer_load_dwordx4 %[a0], %[ws], s61 offen offset:0 sc1 lds\n\t" \
      "buffer_load_dwordx4 %[a0], %[ws], s61 offen offset:1024 sc1 lds\n\t" \
      "buffer_load_dwordx4 %[a0], %[ws], s61 offen offset:2048 sc1 lds\n\t" \
      "buffer_load_dwordx4 %[a0], %[ws], s61 offen offset:3072 sc1 lds\n\t" \
      "s_add_u32 m0, %[j9], 8192\n\t" \
      "s_nop 0\n\t" \
      "buffer_load_dwordx4 %[a4], %[ws], s61 offen offset:0 sc1 lds\n\t" \
      "s_add_u32 s66, %[sp], 2322432\n\t" \
      "s_buffer_load_dword s63, %[w1], s66\n\t" \
      "s_buffer_load_dword s63, %[w1], s66 offset:64\n\t" \
      "s_add_u32 s66, %[sp], 2433024\n\t" \
      "s_buffer_load_dword s63, %[w1], s66\n\t" \
      "s_buffer_load_dword s63, %[w1], s66 offset:64\n\t" \
      "s_waitcnt vmcnt(5)\n\t" \
      "s_barrier\n\t" \
      "s_add_u32 m0, %[j9], 40960\n\t" \
      "s_bfe_u32 s61, s70, 0x50005\n\t" \
      "s_mul_i32 s61, s61, 20480\n\t" \
      "buffer_load_dwordx4 %[a0], %[ws], s61 offen offset:0 sc1 lds\n\t" \
      "buffer_load_dwordx4 %[a0], %[ws], s61 offen offset:1024 sc1 lds\n\t" \
      "buffer_load_dwordx4 %[a0], %[ws], s61 offen offset:2048 sc1 lds\n\t" \
      "buffer_load_dwordx4 %[a0], %[ws], s61 offen offset:3072 sc1 lds\n\t" \
      "s_add_u32 m0, %[j9], 45056\n\t" \
      "s_nop 0\n\t" \
      "buffer_load_dwordx4 %[a4], %[ws], s61 offen offset:0 sc1 lds\n\t" \
      "s_add_u32 m0, %[j9], 73728\n\t" \
      "s_bfe_u32 s60, s70, 0x5000a\n\t" \
      "s_mul_i32 s60, s60, 442368\n\t" \
      "buffer_load_dwordx4 %[b0], %[w1], s60 offen offset:0 sc1 lds\n\t" \
      "buffer_load_dwordx4 %[b1], %[w1], s60 offen offset:1024 sc1 lds\n\t" \
      "buffer_load_dwordx4 %[b2], %[w1], s60 offen offset:2048 sc1 lds\n\t" \
      "buffer_load_dwordx4 %[b3], %[w1], s60 offen offset:3072 sc1 lds\n\t" \
      "s_add_u32 m0, %[j9], 77824\n\t" \
      "s_bfe_u32 s61, s70, 0x5000a\n\t" \
      "s_mul_i32 s61, s61, 20480\n\t" \
      "buffer_load_dwordx4 %[a0], %[ws], s61 offen offset:0 sc1 lds\n\t" \
      "buffer_load_dwordx4 %[a0], %[ws], s61 offen offset:1024 sc1 lds\n\t" \
      "buffer_load_dwordx4 %[a0], %[ws], s61 offen offset:2048 sc1 lds\n\t" \
      "buffer_load_dwordx4 %[a0], %[ws], s61 offen offset:3072 sc1 lds\n\t" \
      "s_add_u32 m0, %[j9], 81920\n\t" \
      "s_nop 0\n\t" \
      "buffer_load_dwordx4 %[a4], %[ws], s61 offen offset:0 sc1 lds\n\t" \
      "s_waitcnt lgkmcnt(0)\n\t"
#define UL_ASM \
      "s_mov_b32 s70, 0xa311020\n\t" \
      "s_mov_b32 s71, 0x14c49cc8\n\t" \
      "s_mov_b32 s72, 0x7b9ab\n\t" \
      "s_cmp_eq_u32 %[cu], 1\n\t" \
      "s_cselect_b32 s70, 0x8328820, s70\n\t" \
      "s_cselect_b32 s71, 0x1aa41d26, s71\n\t" \
      "s_cselect_b32 s72, 0x7b98b, s72\n\t" \
      "s_cmp_eq_u32 %[cu], 2\n\t" \
      "s_cselect_b32 s70, 0x8618402, s70\n\t" \
      "s_cselect_b32 s71, 0x169428e5, s71\n\t" \
      "s_cselect_b32 s72, 0x7b58e, s72\n\t" \
      "s_cmp_eq_u32 %[cu], 3\n\t" \
      "s_cselect_b32 s70, 0xe410460, s70\n\t" \
      "s_cselect_b32 s71, 0x1495a0c5, s71\n\t" \
      "s_cselect_b32 s72, 0x735ec, s72\n\t" \
      "s_waitcnt vmcnt(14)\n\t" \
      "s_barrier\n\t" \
      "s_add_u32 m0, %[j9], 110592\n\t" \
      "s_bfe_u32 s60, s70, 0x5000f\n\t" \
      "s_mul_i32 s60, s60, 442368\n\t" \
      "buffer_load_dwordx4 %[b0], %[w1], s60 offen offset:0 sc1 lds\n\t" \
      "buffer_load_dwordx4 %[b1], %[w1], s60 offen offset:1024 sc1 lds\n\t" \
      "buffer_load_dwordx4 %[b2], %[w1], s60 offen offset:2048 sc1 lds\n\t" \
      "s_add_u32 s66, %[sp], 2543616\n\t" \
      "s_buffer_load_dword s63, %[w1], s66\n\t" \
      "s_buffer_load_dword s63, %[w1], s66 offset:64\n\t" \
      "buffer_load_dwordx4 %[b3], %[w1], s60 offen offset:3072 sc1 lds\n\t" \
      "s_add_u32 s66, %[sp], 2654208\n\t" \
      "s_buffer_load_dword s63, %[w1], s66\n\t" \
      "s_buffer_load_dword s63, %[w1], s66 offset:64\n\t" \
      "s_add_u32 m0, %[j9], 114688\n\t" \
      "s_bfe_u32 s61, s70, 0x5000f\n\t" \
      "s_mul_i32 s61, s61, 20480\n\t" \
      "buffer_load_dwordx4 %[a0], %[ws], s61 offen offset:0 sc1 lds\n\t" \
      "s_add_u32 s66, %[sp], 2764800\n\t" \
      "s_buffer_load_dword s63, %[w1], s66\n\t" \
      "s_buffer_load_dword s63, %[w1], s66 offset:64\n\t" \
      "buffer_load_dwordx4 %[a0], %[ws], s61 offen offset:1024 sc1 lds\n\t" \
      "s_add_u32 s66, %[sp], 2875392\n\t" \
      "s_buffer_load_dword s63, %[w1], s66\n\t" \
      "s_buffer_load_dword s63, %[w1], s66 offset:64\n\t" \
      "buffer_load_dwordx4 %[a0], %[ws], s61 offen offset:2048 sc1 lds\n\t" \
      "buffer_load_dwordx4 %[a0], %[ws], s61 offen offset:3072 sc1 lds\n\t" \
      "s_waitcnt vmcnt(17)\n\t" \
      "s_barrier\n\t" \
      "s_add_u32 m0, %[j9], 118784\n\t" \
      "s_nop 0\n\t" \
      "buffer_load_dwordx4 %[a4], %[ws], s61 offen offset:0 sc1 lds\n\t" \
      "s_add_u32 m0, %[j9], 0\n\t" \
      "s_bfe_u32 s60, s70, 0x50014\n\t" \
      "s_mul_i32 s60, s60, 442368\n\t" \
      "buffer_load_dwordx4 %[b0], %[w1], s60 offen offset:0 sc1 lds\n\t" \
      "buffer_load_dwordx4 %[b1], %[w1], s60 offen offset:1024 sc1 lds\n\t" \
      "s_add_u32 s66, %[sp], 2985984\n\t" \
      "s_buffer_load_dword s63, %[w1], s66\n\t" \
      "s_buffer_load_dword s63, %[w1], s66 offset:64\n\t" \
      "buffer_load_dwordx4 %[b2], %[w1], s60 offen offset:2048 sc1 lds\n\t" \
      "s_add_u32 s66, %[sp], 3096576\n\t" \
      "s_buffer_load_dword s63, %[w1], s66\n\t" \
      "s_buffer_load_dword s63, %[w1], s66 offset:64\n\t" \
      "buffer_load_dwordx4 %[b3], %[w1], s60 offen offset:3072 sc1 lds\n\t" \
      "s_add_u32 s66, %[sp], 3207168\n\t" \
      "s_buffer_load_dword s63, %[w1], s66\n\t" \
      "s_buffer_load_dword s63, %[w1], s66 offset:64\n\t" \
      "s_add_u32 m0, %[j9], 4096\n\t" \
      "s_bfe_u32 s61, s70, 0x50014\n\t" \
      "s_mul_i32 s61, s61, 20480\n\t" \
      "buffer_load_dwordx4 %[a0], %[ws], s61 offen offset:0 sc1 lds\n\t" \
      "s_add_u32 s66, %[sp], 3317760\n\t" \
      "s_buffer_load_dword s63, %[w1], s66\n\t" \
      "s_buffer_load_dword s63, %[w1], s66 offset:64\n\t" \
      "buffer_load_dwordx4 %[a0], %[ws], s61 offen offset:1024 sc1 lds\n\t" \
      "buffer_load_dwordx4 %[a0], %[ws], s61 offen offset:2048 sc1 lds\n\t" \
      "s_waitcnt vmcnt(16)\n\t" \
      "s_barrier\n\t" \
      "buffer_load_dwordx4 %[a0], %[ws], s61 offen offset:3072 sc1 lds\n\t" \
      "s_add_u32 m0, %[j9], 8192\n\t" \
      "s_nop 0\n\t" \
      "buffer_load_dwordx4 %[a4], %[ws], s61 offen offset:0 sc1 lds\n\t" \
      "s_add_u32 m0, %[j9], 36864\n\t" \
      "s_bfe_u32 s60, s70, 0x50019\n\t" \
      "s_mul_i32 s60, s60, 442368\n\t" \
      "buffer_load_dwordx4 %[b0], %[w1], s60 offen offset:0 sc1 lds\n\t" \
      "s_add_u32 s66, %[sp], 3428352\n\t" \
      "s_buffer_load_dword s63, %[w1], s66\n\t" \
      "s_buffer_load_dword s63, %[w1], s66 offset:64\n\t" \
      "buffer_load_dwordx4 %[b1], %[w1], s60 offen offset:1024 sc1 lds\n\t" \
      "s_add_u32 s66, %[sp], 3538944\n\t" \
      "s_buffer_load_dword s63, %[w1], s66\n\t" \
      "s_buffer_load_dword s63, %[w1], s66 offset:64\n\t" \
      "buffer_load_dwordx4 %[b2], %[w1], s60 offen offset:2048 sc1 lds\n\t" \
      "s_add_u32 s66, %[sp], 3649536\n\t" \
      "s_buffer_load_dword s63, %[w1], s66\n\t" \
      "s_buffer_load_dword s63, %[w1], s66 offset:64\n\t" \
      "buffer_load_dwordx4 %[b3], %[w1], s60 offen offset:3072 sc1 lds\n\t" \
      "s_add_u32 s66, %[sp], 3760128\n\t" \
      "s_buffer_load_dword s63, %[w1], s66\n\t" \
      "s_buffer_load_dword s63, %[w1], s66 offset:64\n\t" \
      "s_add_u32 m0, %[j9], 40960\n\t" \
      "s_bfe_u32 s61, s70, 0x50019\n\t" \
      "s_mul_i32 s61, s61, 20480\n\t" \
      "buffer_load_dwordx4 %[a0], %[ws], s61 offen offset:0 sc1 lds\n\t" \
      "buffer_load_dwordx4 %[a0], %[ws], s61 offen offset:1024 sc1 lds\n\t" \
      "buffer_load_dwordx4 %[a0], %[ws], s61 offen offset:2048 sc1 lds\n\t" \
      "s_waitcnt vmcnt(16)\n\t" \
      "s_barrier\n\t" \
      "buffer_load_dwordx4 %[a0], %[ws], s61 offen offset:3072 sc1 lds\n\t" \
      "s_add_u32 m0, %[j9], 45056\n\t" \
      "s_nop 0\n\t" \
      "buffer_load_dwordx4 %[a4], %[ws], s61 offen offset:0 sc1 lds\n\t" \
      "s_add_u32 m0, %[j9], 73728\n\t" \
      "s_bfe_u32 s60, s71, 0x50000\n\t" \
      "s_mul_i32 s60, s60, 442368\n\t" \
      "buffer_load_dwordx4 %[b0], %[w1], s60 offen offset:0 sc1 lds\n\t" \
      "s_add_u32 s66, %[sp], 3870720\n\t" \
      "s_buffer_load_dword s63, %[w1], s66\n\t" \
      "s_buffer_load_dword s63, %[w1], s66 offset:64\n\t" \
      "buffer_load_dwordx4 %[b1], %[w1], s60 offen offset:1024 sc1 lds\n\t" \
      "s_add_u32 s66, %[sp], 3981312\n\t" \
      "s_buffer_load_dword s63, %[w1], s66\n\t" \
      "s_buffer_load_dword s63, %[w1], s66 offset:64\n\t" \
      "buffer_load_dwordx4 %[b2], %[w1], s60 offen offset:2048 sc1 lds\n\t" \
      "s_add_u32 s66, %[sp], 4091904\n\t" \
      "s_buffer_load_dword s63, %[w1], s66\n\t" \
      "s_buffer_load_dword s63, %[w1], s66 offset:64\n\t" \
      "buffer_load_dwordx4 %[b3], %[w1], s60 offen offset:3072 sc1 lds\n\t" \
      "s_add_u32 s66, %[sp], 4202496\n\t" \
      "s_buffer_load_dword s63, %[w1], s66\n\t" \
      "s_buffer_load_dword s63, %[w1], s66 offset:64\n\t" \
      "s_add_u32 m0, %[j9], 77824\n\t" \
      "s_bfe_u32 s61, s71, 0x50000\n\t" \
      "s_mul_i32 s61, s61, 20480\n\t" \
      "buffer_load_dwordx4 %[a0], %[ws], s61 offen offset:0 sc1 lds\n\t" \
      "buffer_load_dwordx4 %[a0], %[ws], s61 offen offset:1024 sc1 lds\n\t" \
      "s_waitcnt vmcnt(15)\n\t" \
      "s_barrier\n\t" \
      "buffer_load_dwordx4 %[a0], %[ws], s61 offen offset:2048 sc1 lds\n\t" \
      "buffer_load_dwordx4 %[a0], %[ws], s61 offen offset:3072 sc1 lds\n\t" \
      "s_add_u32 m0, %[j9], 81920\n\t" \
      "s_nop 0\n\t" \
      "buffer_load_dwordx4 %[a4], %[ws], s61 offen offset:0 sc1 lds\n\t" \
      "s_add_u32 s66, %[sp], 4313088\n\t" \
      "s_buffer_load_dword s63, %[w1], s66\n\t" \
      "s_buffer_load_dword s63, %[w1], s66 offset:64\n\t" \
      "s_add_u32 m0, %[j9], 110592\n\t" \
      "s_bfe_u32 s60, s71, 0x50005\n\t" \
      "s_mul_i32 s60, s60, 442368\n\t" \
      "buffer_load_dwordx4 %[b0], %[w1], s60 offen offset:0 sc1 lds\n\t" \
      "s_add_u32 s66, %[sp], 4423680\n\t" \
      "s_buffer_load_dword s63, %[w1], s66\n\t" \
      "s_buffer_load_dword s63, %[w1], s66 offset:64\n\t" \
      "buffer_load_dwordx4 %[b1], %[w1], s60 offen offset:1024 sc1 lds\n\t" \
      "s_add_u32 s66, %[sp], 4534272\n\t" \
      "s_buffer_load_dword s63, %[w1], s66\n\t" \
      "s_buffer_load_dword s63, %[w1], s66 offset:64\n\t" \
      "buffer_load_dwordx4 %[b2], %[w1], s60 offen offset:2048 sc1 lds\n\t" \
      "s_add_u32 s66, %[sp], 4644864\n\t" \
      "s_buffer_load_dword s63, %[w1], s66\n\t" \
      "s_buffer_load_dword s63, %[w1], s66 offset:64\n\t" \
      "buffer_load_dwordx4 %[b3], %[w1], s60 offen offset:3072 sc1 lds\n\t" \
      "s_add_u32 m0, %[j9], 114688\n\t" \
      "s_bfe_u32 s61, s71, 0x50005\n\t" \
      "s_mul_i32 s61, s61, 20480\n\t" \
      "buffer_load_dwordx4 %[a0], %[ws], s61 offen offset:0 sc1 lds\n\t" \
      "s_waitcnt vmcnt(14)\n\t" \
      "s_barrier\n\t" \
      "buffer_load_dwordx4 %[a0], %[ws], s61 offen offset:1024 sc1 lds\n\t" \
      "buffer_load_dwordx4 %[a0], %[ws], s61 offen offset:2048 sc1 lds\n\t" \
      "buffer_load_dwordx4 %[a0], %[ws], s61 offen offset:3072 sc1 lds\n\t" \
      "s_add_u32 s66, %[sp], 4755456\n\t" \
      "s_buffer_load_dword s63, %[w1], s66\n\t" \
      "s_buffer_load_dword s63, %[w1], s66 offset:64\n\t" \
      "s_add_u32 m0, %[j9], 118784\n\t" \
      "s_nop 0\n\t" \
      "buffer_load_dwordx4 %[a4], %[ws], s61 offen offset:0 sc1 lds\n\t" \
      "s_add_u32 s66, %[sp], 4866048\n\t" \
      "s_buffer_load_dword s63, %[w1], s66\n\t" \
      "s_buffer_load_dword s63, %[w1], s66 offset:64\n\t" \
      "s_add_u32 m0, %[j9], 0\n\t" \
      "s_bfe_u32 s60, s71, 0x5000a\n\t" \
      "s_mul_i32 s60, s60, 442368\n\t" \
      "buffer_load_dwordx4 %[b0], %[w1], s60 offen offset:0 sc1 lds\n\t" \
      "s_add_u32 s66, %[sp], 4976640\n\t" \
      "s_buffer_load_dword s63, %[w1], s66\n\t" \
      "s_buffer_load_dword s63, %[w1], s66 offset:64\n\t" \
      "buffer_load_dwordx4 %[b1], %[w1], s60 offen offset:1024 sc1 lds\n\t" \
      "s_add_u32 s66, %[sp], 5087232\n\t" \
      "s_buffer_load_dword s63, %[w1], s66\n\t" \
      "s_buffer_load_dword s63, %[w1], s66 offset:64\n\t" \
      "buffer_load_dwordx4 %[b2], %[w1], s60 offen offset:2048 sc1 lds\n\t" \
      "buffer_load_dwordx4 %[b3], %[w1], s60 offen offset:3072 sc1 lds\n\t" \
      "s_add_u32 m0, %[j9], 4096\n\t" \
      "s_bfe_u32 s61, s71, 0x5000a\n\t" \
      "s_mul_i32 s61, s61, 20480\n\t" \
      "buffer_load_dwordx4 %[a0], %[ws], s61 offen offset:0 sc1 lds\n\t" \
      "s_waitcnt vmcnt(14)\n\t" \
      "s_barrier\n\t" \
      "buffer_load_dwordx4 %[a0], %[ws], s61 offen offset:1024 sc1 lds\n\t" \
      "buffer_load_dwordx4 %[a0], %[ws], s61 offen offset:2048 sc1 lds\n\t" \
      "buffer_load_dwordx4 %[a0], %[ws], s61 offen offset:3072 sc1 lds\n\t" \
      "s_add_u32 s66, %[sp], 5197824\n\t" \
      "s_buffer_load_dword s63, %[w1], s66\n\t" \
      "s_buffer_load_dword s63, %[w1], s66 offset:64\n\t" \
      "s_add_u32 m0, %[j9], 8192\n\t" \
      "s_nop 0\n\t" \
      "buffer_load_dwordx4 %[a4], %[ws], s61 offen offset:0 sc1 lds\n\t" \
      "s_add_u32 s66, %[sp], 5308416\n\t" \
      "s_buffer_load_dword s63, %[w1], s66\n\t" \
      "s_buffer_load_dword s63, %[w1], s66 offset:64\n\t" \
      "s_add_u32 m0, %[j9], 36864\n\t" \
      "s_bfe_u32 s60, s71, 0x5000f\n\t" \
      "s_mul_i32 s60, s60, 442368\n\t" \
      "buffer_load_dwordx4 %[b0], %[w1], s60 offen offset:0 sc1 lds\n\t" \
      "s_add_u32 s66, %[sp], 5419008\n\t" \
      "s_buffer_load_dword s63, %[w1], s66\n\t" \
      "s_buffer_load_dword s63, %[w1], s66 offset:64\n\t" \
      "buffer_load_dwordx4 %[b1], %[w1], s60 offen offset:1024 sc1 lds\n\t" \
      "s_add_u32 s66, %[sp], 5529600\n\t" \
      "s_buffer_load_dword s63, %[w1], s66\n\t" \
      "s_buffer_load_dword s63, %[w1], s66 offset:64\n\t" \
      "buffer_load_dwordx4 %[b2], %[w1], s60 offen offset:2048 sc1 lds\n\t" \
      "buffer_load_dwordx4 %[b3], %[w1], s60 offen offset:3072 sc1 lds\n\t" \
      "s_waitcnt vmcnt(13)\n\t" \
      "s_barrier\n\t" \
      "s_add_u32 m0, %[j9], 40960\n\t" \
      "s_bfe_u32 s61, s71, 0x5000f\n\t" \
      "s_mul_i32 s61, s61, 20480\n\t" \
      "buffer_load_dwordx4 %[a0], %[ws], s61 offen offset:0 sc1 lds\n\t" \
      "buffer_load_dwordx4 %[a0], %[ws], s61 offen offset:1024 sc1 lds\n\t" \
      "buffer_load_dwordx4 %[a0], %[ws], s61 offen offset:2048 sc1 lds\n\t" \
      "s_add_u32 s66, %[sp], 5640192\n\t" \
      "s_buffer_load_dword s63, %[w1], s66\n\t" \
      "s_buffer_load_dword s63, %[w1], s66 offset:64\n\t" \
      "buffer_load_dwordx4 %[a0], %[ws], s61 offen offset:3072 sc1 lds\n\t" \
      "s_add_u32 s66, %[sp], 5750784\n\t" \
      "s_buffer_load_dword s63, %[w1], s66\n\t" \
      "s_buffer_load_dword s63, %[w1], s66 offset:64\n\t" \
      "s_add_u32 m0, %[j9], 45056\n\t" \
      "s_nop 0\n\t" \
      "buffer_load_dwordx4 %[a4], %[ws], s61 offen offset:0 sc1 lds\n\t" \
      "s_add_u32 s66, %[sp], 5861376\n\t" \
      "s_buffer_load_dword s63, %[w1], s66\n\t" \
      "s_buffer_load_dword s63, %[w1], s66 offset:64\n\t" \
      "s_add_u32 m0, %[j9], 73728\n\t" \
      "s_bfe_u32 s60, s71, 0x50014\n\t" \
      "s_mul_i32 s60, s60, 442368\n\t" \
      "buffer_load_dwordx4 %[b0], %[w1], s60 offen offset:0 sc1 lds\n\t" \
      "s_add_u32 s66, %[sp], 5971968\n\t" \
      "s_buffer_load_dword s63, %[w1], s66\n\t" \
      "s_buffer_load_dword s63, %[w1], s66 offset:64\n\t" \
      "buffer_load_dwordx4 %[b1], %[w1], s60 offen offset:1024 sc1 lds\n\t" \
      "buffer_load_dwordx4 %[b2], %[w1], s60 offen offset:2048 sc1 lds\n\t" \
      "s_waitcnt vmcnt(12)\n\t" \
      "s_barrier\n\t" \
      "buffer_load_dwordx4 %[b3], %[w1], s60 offen offset:3072 sc1 lds\n\t" \
      "s_add_u32 m0, %[j9], 77824\n\t" \
      "s_bfe_u32 s61, s71, 0x50014\n\t" \
      "s_mul_i32 s61, s61, 20480\n\t" \
      "buffer_load_dwordx4 %[a0], %[ws], s61 offen offset:0 sc1 lds\n\t" \
      "buffer_load_dwordx4 %[a0], %[ws], s61 offen offset:1024 sc1 lds\n\t" \
      "s_add_u32 s66, %[sp], 6082560\n\t" \
      "s_buffer_load_dword s63, %[w1], s66\n\t" \
      "s_buffer_load_dword s63, %[w1], s66 offset:64\n\t" \
      "buffer_load_dwordx4 %[a0], %[ws], s61 offen offset:2048 sc1 lds\n\t" \
      "s_add_u32 s66, %[sp], 6193152\n\t" \
      "s_buffer_load_dword s63, %[w1], s66\n\t" \
      "s_buffer_load_dword s63, %[w1], s66 offset:64\n\t" \
      "buffer_load_dwordx4 %[a0], %[ws], s61 offen offset:3072 sc1 lds\n\t" \
      "s_add_u32 s66, %[sp], 6303744\n\t" \
      "s_buffer_load_dword s63, %[w1], s66\n\t" \
      "s_buffer_load_dword s63, %[w1], s66 offset:64\n\t" \
      "s_add_u32 m0, %[j9], 81920\n\t" \
      "s_nop 0\n\t" \
      "buffer_load_dwordx4 %[a4], %[ws], s61 offen offset:0 sc1 lds\n\t" \
      "s_add_u32 s66, %[sp], 6414336\n\t" \
      "s_buffer_load_dword s63, %[w1], s66\n\t" \
      "s_buffer_load_dword s63, %[w1], s66 offset:64\n\t" \
      "s_add_u32 m0, %[j9], 110592\n\t" \
      "s_bfe_u32 s60, s71, 0x50019\n\t" \
      "s_mul_i32 s60, s60, 442368\n\t" \
      "buffer_load_dwordx4 %[b0], %[w1], s60 offen offset:0 sc1 lds\n\t" \
      "buffer_load_dwordx4 %[b1], %[w1], s60 offen offset:1024 sc1 lds\n\t" \
      "buffer_load_dwordx4 %[b2], %[w1], s60 offen offset:2048 sc1 lds\n\t" \
      "s_waitcnt vmcnt(12)\n\t" \
      "s_barrier\n\t" \
      "buffer_load_dwordx4 %[b3], %[w1], s60 offen offset:3072 sc1 lds\n\t" \
      "s_add_u32 m0, %[j9], 114688\n\t" \
      "s_bfe_u32 s61, s71, 0x50019\n\t" \
      "s_mul_i32 s61, s61, 20480\n\t" \
      "buffer_load_dwordx4 %[a0], %[ws], s61 offen offset:0 sc1 lds\n\t" \
      "buffer_load_dwordx4 %[a0], %[ws], s61 offen offset:1024 sc1 lds\n\t" \
      "s_add_u32 s66, %[sp], 6524928\n\t" \
      "s_buffer_load_dword s63, %[w1], s66\n\t" \
      "s_buffer_load_dword s63, %[w1], s66 offset:64\n\t" \
      "buffer_load_dwordx4 %[a0], %[ws], s61 offen offset:2048 sc1 lds\n\t" \
      "s_add_u32 s66, %[sp], 6635520\n\t" \
      "s_buffer_load_dword s63, %[w1], s66\n\t" \
      "s_buffer_load_dword s63, %[w1], s66 offset:64\n\t" \
      "buffer_load_dwordx4 %[a0], %[ws], s61 offen offset:3072 sc1 lds\n\t" \
      "s_add_u32 s66, %[sp], 6746112\n\t" \
      "s_buffer_load_dword s63, %[w1], s66\n\t" \
      "s_buffer_load_dword s63, %[w1], s66 offset:64\n\t" \
      "s_add_u32 m0, %[j9], 118784\n\t" \
      "s_nop 0\n\t" \
      "buffer_load_dwordx4 %[a4], %[ws], s61 offen offset:0 sc1 lds\n\t" \
      "s_add_u32 s66, %[sp], 6856704\n\t" \
      "s_buffer_load_dword s63, %[w1], s66\n\t" \
      "s_buffer_load_dword s63, %[w1], s66 offset:64\n\t" \
      "s_add_u32 m0, %[j9], 0\n\t" \
      "s_bfe_u32 s60, s72, 0x50000\n\t" \
      "s_mul_i32 s60, s60, 442368\n\t" \
      "buffer_load_dwordx4 %[b0], %[w1], s60 offen offset:0 sc1 lds\n\t" \
      "buffer_load_dwordx4 %[b1], %[w1], s60 offen offset:1024 sc1 lds\n\t" \
      "s_waitcnt vmcnt(11)\n\t" \
      "s_barrier\n\t" \
      "buffer_load_dwordx4 %[b2], %[w1], s60 offen offset:2048 sc1 lds\n\t" \
      "buffer_load_dwordx4 %[b3], %[w1], s60 offen offset:3072 sc1 lds\n\t" \
      "s_add_u32 m0, %[j9], 4096\n\t" \
      "s_bfe_u32 s61, s72, 0x50000\n\t" \
      "s_mul_i32 s61, s61, 20480\n\t" \
      "buffer_load_dwordx4 %[a0], %[ws], s61 offen offset:0 sc1 lds\n\t" \
      "s_add_u32 s66, %[sp], 6967296\n\t" \
      "s_buffer_load_dword s63, %[w1], s66\n\t" \
      "s_buffer_load_dword s63, %[w1], s66 offset:64\n\t" \
      "buffer_load_dwordx4 %[a0], %[ws], s61 offen offset:1024 sc1 lds\n\t" \
      "buffer_load_dwordx4 %[a0], %[ws], s61 offen offset:2048 sc1 lds\n\t" \
      "buffer_load_dwordx4 %[a0], %[ws], s61 offen offset:3072 sc1 lds\n\t" \
      "s_add_u32 m0, %[j9], 8192\n\t" \
      "s_nop 0\n\t" \
      "buffer_load_dwordx4 %[a4], %[ws], s61 offen offset:0 sc1 lds\n\t" \
      "s_add_u32 m0, %[j9], 36864\n\t" \
      "s_bfe_u32 s60, s72, 0x50005\n\t" \
      "s_mul_i32 s60, s60, 442368\n\t" \
      "buffer_load_dwordx4 %[b0], %[w1], s60 offen offset:0 sc1 lds\n\t" \
      "s_waitcnt vmcnt(10)\n\t" \
      "s_barrier\n\t" \
      "buffer_load_dwordx4 %[b1], %[w1], s60 offen offset:1024 sc1 lds\n\t" \
      "buffer_load_dwordx4 %[b2], %[w1], s60 offen offset:2048 sc1 lds\n\t" \
      "buffer_load_dwordx4 %[b3], %[w1], s60 offen offset:3072 sc1 lds\n\t" \
      "s_add_u32 m0, %[j9], 40960\n\t" \
      "s_bfe_u32 s61, s72, 0x50005\n\t" \
      "s_mul_i32 s61, s61, 20480\n\t" \
      "buffer_load_dwordx4 %[a0], %[ws], s61 offen offset:0 sc1 lds\n\t" \
      "buffer_load_dwordx4 %[a0], %[ws], s61 offen offset:1024 sc1 lds\n\t" \
      "buffer_load_dwordx4 %[a0], %[ws], s61 offen offset:2048 sc1 lds\n\t" \
      "buffer_load_dwordx4 %[a0], %[ws], s61 offen offset:3072 sc1 lds\n\t" \
      "s_add_u32 m0, %[j9], 45056\n\t" \
      "s_nop 0\n\t" \
      "buffer_load_dwordx4 %[a4], %[ws], s61 offen offset:0 sc1 lds\n\t" \
      "s_add_u32 m0, %[j9], 73728\n\t" \
      "s_bfe_u32 s60, s72, 0x5000a\n\t" \
      "s_mul_i32 s60, s60, 442368\n\t" \
      "buffer_load_dwordx4 %[b0], %[w1], s60 offen offset:0 sc1 lds\n\t" \
      "s_waitcnt vmcnt(10)\n\t" \
      "s_barrier\n\t" \
      "buffer_load_dwordx4 %[b1], %[w1], s60 offen offset:1024 sc1 lds\n\t" \
      "buffer_load_dwordx4 %[b2], %[w1], s60 offen offset:2048 sc1 lds\n\t" \
      "buffer_load_dwordx4 %[b3], %[w1], s60 offen offset:3072 sc1 lds\n\t" \
      "s_add_u32 m0, %[j9], 77824\n\t" \
      "s_bfe_u32 s61, s72, 0x5000a\n\t" \
      "s_mul_i32 s61, s61, 20480\n\t" \
      "buffer_load_dwordx4 %[a0], %[ws], s61 offen offset:0 sc1 lds\n\t" \
      "buffer_load_dwordx4 %[a0], %[ws], s61 offen offset:1024 sc1 lds\n\t" \
      "buffer_load_dwordx4 %[a0], %[ws], s61 offen offset:2048 sc1 lds\n\t" \
      "buffer_load_dwordx4 %[a0], %[ws], s61 offen offset:3072 sc1 lds\n\t" \
      "s_add_u32 m0, %[j9], 81920\n\t" \
      "s_nop 0\n\t" \
      "buffer_load_dwordx4 %[a4], %[ws], s61 offen offset:0 sc1 lds\n\t" \
      "s_waitcnt vmcnt(9)\n\t" \
      "s_barrier\n\t" \
      "s_add_u32 m0, %[j9], 110592\n\t" \
      "s_bfe_u32 s60, s72, 0x5000f\n\t" \
      "s_mul_i32 s60, s60, 442368\n\t" \
      "buffer_load_dwordx4 %[b0], %[w1], s60 offen offset:0 sc1 lds\n\t" \
      "buffer_load_dwordx4 %[b1], %[w1], s60 offen offset:1024 sc1 lds\n\t" \
      "buffer_load_dwordx4 %[b2], %[w1], s60 offen offset:2048 sc1 lds\n\t" \
      "buffer_load_dwordx4 %[b3], %[w1], s60 offen offset:3072 sc1 lds\n\t" \
      "s_add_u32 m0, %[j9], 114688\n\t" \
      "s_bfe_u32 s61, s72, 0x5000f\n\t" \
      "s_mul_i32 s61, s61, 20480\n\t" \
      "buffer_load_dwordx4 %[a0], %[ws], s61 offen offset:0 sc1 lds\n\t" \
      "buffer_load_dwordx4 %[a0], %[ws], s61 offen offset:1024 sc1 lds\n\t" \
      "buffer_load_dwordx4 %[a0], %[ws], s61 offen offset:2048 sc1 lds\n\t" \
      "buffer_load_dwordx4 %[a0], %[ws], s61 offen offset:3072 sc1 lds\n\t" \
      "s_add_u32 m0, %[j9], 118784\n\t" \
      "s_nop 0\n\t" \
      "buffer_load_dwordx4 %[a4], %[ws], s61 offen offset:0 sc1 lds\n\t" \
      "s_waitcnt lgkmcnt(0)\n\t"
#define YL_ASM \
      "s_mov_b32 s62, 0\n\t" \
      "s_movk_i32 s63, 18432\n\t" \
      "s_lshl_b32 s68, %[j2], 1\n\t" \
      "s_waitcnt vmcnt(9)\n\t" \
      "s_barrier\n\t" \
      "s_add_u32 m0, s68, 147456\n\t" \
      "s_nop 0\n\t" \
      "buffer_load_dwordx4 %[yx0], %[x], 0 offen offset:0 sc1 lds\n\t" \
      "buffer_load_dwordx4 %[yx1], %[x], 0 offen offset:1024 sc1 lds\n\t" \
      "buffer_load_dwordx4 %[yx0], %[x], s63 offen offset:2048 sc1 lds\n\t" \
      "buffer_load_dwordx4 %[yx1], %[x], s63 offen offset:3072 sc1 lds\n\t" \
      "s_mov_b32 s70, 0x2726b16a\n\t" \
      "s_mov_b32 s71, 0x2b48c1ee\n\t" \
      "s_mov_b32 s72, 0x1ace2f6\n\t" \
      "s_mov_b32 s73, 0x8940c41\n\t" \
      "s_mov_b32 s74, 0x1cc5\n\t" \
      "s_cmp_eq_u32 %[cy], 1\n\t" \
      "s_cselect_b32 s70, 0x1ee6b16a, s70\n\t" \
      "s_cselect_b32 s71, 0x2728c2b4, s71\n\t" \
      "s_cselect_b32 s72, 0x1ace2f6, s72\n\t" \
      "s_cselect_b32 s73, 0xc520c41, s73\n\t" \
      "s_cselect_b32 s74, 0x2507, s74\n\t" \
      "s_cmp_eq_u32 %[cy], 2\n\t" \
      "s_cselect_b32 s70, 0x1ac5a9ee, s70\n\t" \
      "s_cselect_b32 s71, 0x272bda30, s71\n\t" \
      "s_cselect_b32 s72, 0x9ace2b4, s72\n\t" \
      "s_cselect_b32 s73, 0xc310405, s73\n\t" \
      "s_cselect_b32 s74, 0x2507, s74\n\t" \
      "s_cmp_eq_u32 %[cy], 3\n\t" \
      "s_cselect_b32 s70, 0x1ac8c16a, s70\n\t" \
      "s_cselect_b32 s71, 0x3389c9ee, s71\n\t" \
      "s_cselect_b32 s72, 0x1abdab4, s72\n\t" \
      "s_cselect_b32 s73, 0x8311cc1, s73\n\t" \
      "s_cselect_b32 s74, 0x2505, s74\n\t" \
      "s_waitcnt vmcnt(4)\n\t" \
      "s_barrier\n\t" \
      "s_add_u32 m0, s68, 0\n\t" \
      "s_bfe_u32 s60, s70, 0x50000\n\t" \
      "s_mul_i32 s60, s60, 262144\n\t" \
      "buffer_load_dwordx4 %[yb0], %[w2], s60 offen offset:0 sc1 lds\n\t" \
      "buffer_load_dwordx4 %[yb1], %[w2], s60 offen offset:1024 sc1 lds\n\t" \
      "buffer_load_dwordx4 %[yb2], %[w2], s60 offen offset:2048 sc1 lds\n\t" \
      "buffer_load_dwordx4 %[yb3], %[w2], s60 offen offset:3072 sc1 lds\n\t" \
      "s_barrier\n\t" \
      "s_waitcnt vmcnt(4)\n\t" \
      "s_barrier\n\t" \
      "s_add_u32 m0, s68, 24576\n\t" \
      "s_bfe_u32 s60, s70, 0x50005\n\t" \
      "s_mul_i32 s60, s60, 262144\n\t" \
      "buffer_load_dwordx4 %[yb0], %[w2], s60 offen offset:0 sc1 lds\n\t" \
      "buffer_load_dwordx4 %[yb1], %[w2], s60 offen offset:1024 sc1 lds\n\t" \
      "buffer_load_dwordx4 %[yb2], %[w2], s60 offen offset:2048 sc1 lds\n\t" \
      "buffer_load_dwordx4 %[yb3], %[w2], s60 offen offset:3072 sc1 lds\n\t" \
      "s_add_u32 m0, s68, 49152\n\t" \
      "s_bfe_u32 s60, s70, 0x5000a\n\t" \
      "s_mul_i32 s60, s60, 262144\n\t" \
      "buffer_load_dwordx4 %[yb0], %[w2], s60 offen offset:0 sc1 lds\n\t" \
      "buffer_load_dwordx4 %[yb1], %[w2], s60 offen offset:1024 sc1 lds\n\t" \
      "buffer_load_dwordx4 %[yb2], %[w2], s60 offen offset:2048 sc1 lds\n\t" \
      "buffer_load_dwordx4 %[yb3], %[w2], s60 offen offset:3072 sc1 lds\n\t" \
      "s_add_u32 m0, s68, 73728\n\t" \
      "s_bfe_u32 s60, s70, 0x5000f\n\t" \
      "s_mul_i32 s60, s60, 262144\n\t" \
      "buffer_load_dwordx4 %[yb0], %[w2], s60 offen offset:0 sc1 lds\n\t" \
      "buffer_load_dwordx4 %[yb1], %[w2], s60 offen offset:1024 sc1 lds\n\t" \
      "buffer_load_dwordx4 %[yb2], %[w2], s60 offen offset:2048 sc1 lds\n\t" \
      "buffer_load_dwordx4 %[yb3], %[w2], s60 offen offset:3072 sc1 lds\n\t" \
      "s_add_u32 m0, s68, 98304\n\t" \
      "s_bfe_u32 s60, s70, 0x50014\n\t" \
      "s_mul_i32 s60, s60, 262144\n\t" \
      "buffer_load_dwordx4 %[yb0], %[w2], s60 offen offset:0 sc1 lds\n\t" \
      "buffer_load_dwordx4 %[yb1], %[w2], s60 offen offset:1024 sc1 lds\n\t" \
      "buffer_load_dwordx4 %[yb2], %[w2], s60 offen offset:2048 sc1 lds\n\t" \
      "buffer_load_dwordx4 %[yb3], %[w2], s60 offen offset:3072 sc1 lds\n\t" \
      "s_add_u32 m0, s68, 122880\n\t" \
      "s_bfe_u32 s60, s70, 0x50019\n\t" \
      "s_mul_i32 s60, s60, 262144\n\t" \
      "buffer_load_dwordx4 %[yb0], %[w2], s60 offen offset:0 sc1 lds\n\t" \
      "buffer_load_dwordx4 %[yb1], %[w2], s60 offen offset:1024 sc1 lds\n\t" \
      "buffer_load_dwordx4 %[yb2], %[w2], s60 offen offset:2048 sc1 lds\n\t" \
      "buffer_load_dwordx4 %[yb3], %[w2], s60 offen offset:3072 sc1 lds\n\t" \
      "s_movk_i32 s63, 30720\n\t" \
      "s_branch 11f\n\t" \
      "10:\n\t" \
      "v_cmp_ne_u32 vcc, %[tag], %[fv]\n\t" \
      "s_add_u32 s62, s62, 1\n\t" \
      "s_cbranch_vccz 110f\n\t" \
      "s_cmp_gt_u32 s62, 0x20000\n\t" \
      "s_cbranch_scc1 110f\n\t" \
      "11:\n\t" \
      "buffer_load_dword %[fv], %[f3], %[ws], 0 offen sc1\n\t" \
      "s_waitcnt vmcnt(0)\n\t" \
      "s_branch 10b\n\t" \
      "110:\n\t" \
      "s_add_u32 m0, %[j2], 16384\n\t" \
      "s_bfe_u32 s61, s70, 0x50000\n\t" \
      "s_mul_i32 s61, s61, 8192\n\t" \
      "buffer_load_dwordx4 %[ya], %[ws], s61 offen offset:0 sc1 lds\n\t" \
      "buffer_load_dwordx4 %[ya], %[ws], s61 offen offset:1024 sc1 lds\n\t" \
      "s_add_u32 m0, %[j2], 40960\n\t" \
      "s_bfe_u32 s61, s70, 0x50005\n\t" \
      "s_mul_i32 s61, s61, 8192\n\t" \
      "buffer_load_dwordx4 %[ya], %[ws], s61 offen offset:0 sc1 lds\n\t" \
      "buffer_load_dwordx4 %[ya], %[ws], s61 offen offset:1024 sc1 lds\n\t" \
      "s_add_u32 m0, %[j2], 65536\n\t" \
      "s_bfe_u32 s61, s70, 0x5000a\n\t" \
      "s_mul_i32 s61, s61, 8192\n\t" \
      "buffer_load_dwordx4 %[ya], %[ws], s61 offen offset:0 sc1 lds\n\t" \
      "buffer_load_dwordx4 %[ya], %[ws], s61 offen offset:1024 sc1 lds\n\t" \
      "s_add_u32 m0, %[j2], 90112\n\t" \
      "s_bfe_u32 s61, s70, 0x5000f\n\t" \
      "s_mul_i32 s61, s61, 8192\n\t" \
      "buffer_load_dwordx4 %[ya], %[ws], s61 offen offset:0 sc1 lds\n\t" \
      "buffer_load_dwordx4 %[ya], %[ws], s61 offen offset:1024 sc1 lds\n\t" \
      "s_waitcnt vmcnt(4)\n\t" \
      "s_barrier\n\t" \
      "s_add_u32 m0, %[j2], 114688\n\t" \
      "s_bfe_u32 s61, s70, 0x50014\n\t" \
      "s_mul_i32 s61, s61, 8192\n\t" \
      "buffer_load_dwordx4 %[ya], %[ws], s61 offen offset:0 sc1 lds\n\t" \
      "buffer_load_dwordx4 %[ya], %[ws], s61 offen offset:1024 sc1 lds\n\t" \
      "s_barrier\n\t" \
      "s_add_u32 m0, %[j2], 139264\n\t" \
      "s_bfe_u32 s61, s70, 0x50019\n\t" \
      "s_mul_i32 s61, s61, 8192\n\t" \
      "buffer_load_dwordx4 %[ya], %[ws], s61 offen offset:0 sc1 lds\n\t" \
      "buffer_load_dwordx4 %[ya], %[ws], s61 offen offset:1024 sc1 lds\n\t" \
      "s_waitcnt vmcnt(6)\n\t" \
      "s_barrier\n\t" \
      "s_add_u32 m0, %[j2], 16384\n\t" \
      "s_bfe_u32 s61, s71, 0x50000\n\t" \
      "s_mul_i32 s61, s61, 8192\n\t" \
      "buffer_load_dwordx4 %[ya], %[ws], s61 offen offset:0 sc1 lds\n\t" \
      "buffer_load_dwordx4 %[ya], %[ws], s61 offen offset:1024 sc1 lds\n\t" \
      "s_add_u32 m0, s68, 0\n\t" \
      "s_bfe_u32 s60, s71, 0x50000\n\t" \
      "s_mul_i32 s60, s60, 262144\n\t" \
      "buffer_load_dwordx4 %[yb0], %[w2], s60 offen offset:0 sc1 lds\n\t" \
      "buffer_load_dwordx4 %[yb1], %[w2], s60 offen offset:1024 sc1 lds\n\t" \
      "buffer_load_dwordx4 %[yb2], %[w2], s60 offen offset:2048 sc1 lds\n\t" \
      "s_waitcnt vmcnt(9)\n\t" \
      "s_barrier\n\t" \
      "buffer_load_dwordx4 %[yb3], %[w2], s60 offen offset:3072 sc1 lds\n\t" \
      "s_add_u32 m0, %[j2], 40960\n\t" \
      "s_bfe_u32 s61, s71, 0x50005\n\t" \
      "s_mul_i32 s61, s61, 8192\n\t" \
      "buffer_load_dwordx4 %[ya], %[ws], s61 offen offset:0 sc1 lds\n\t" \
      "buffer_load_dwordx4 %[ya], %[ws], s61 offen offset:1024 sc1 lds\n\t" \
      "s_add_u32 m0, s68, 24576\n\t" \
      "s_bfe_u32 s60, s71, 0x50005\n\t" \
      "s_mul_i32 s60, s60, 262144\n\t" \
      "buffer_load_dwordx4 %[yb0], %[w2], s60 offen offset:0 sc1 lds\n\t" \
      "buffer_load_dwordx4 %[yb1], %[w2], s60 offen offset:1024 sc1 lds\n\t" \
      "buffer_load_dwordx4 %[yb2], %[w2], s60 offen offset:2048 sc1 lds\n\t" \
      "s_waitcnt vmcnt(13)\n\t" \
      "s_barrier\n\t" \
      "buffer_load_dwordx4 %[yb3], %[w2], s60 offen offset:3072 sc1 lds\n\t" \
      "s_add_u32 m0, %[j2], 65536\n\t" \
      "s_bfe_u32 s61, s71, 0x5000a\n\t" \
      "s_mul_i32 s61, s61, 8192\n\t" \
      "buffer_load_dwordx4 %[ya], %[ws], s61 offen offset:0 sc1 lds\n\t" \
      "buffer_load_dwordx4 %[ya], %[ws], s61 offen offset:1024 sc1 lds\n\t" \
      "s_add_u32 m0, s68, 49152\n\t" \
      "s_bfe_u32 s60, s71, 0x5000a\n\t" \
      "s_mul_i32 s60, s60, 262144\n\t" \
      "buffer_load_dwordx4 %[yb0], %[w2], s60 offen offset:0 sc1 lds\n\t" \
      "buffer_load_dwordx4 %[yb1], %[w2], s60 offen offset:1024 sc1 lds\n\t" \
      "buffer_load_dwordx4 %[yb2], %[w2], s60 offen offset:2048 sc1 lds\n\t" \
      "s_waitcnt vmcnt(17)\n\t" \
      "s_barrier\n\t" \
      "buffer_load_dwordx4 %[yb3], %[w2], s60 offen offset:3072 sc1 lds\n\t" \
      "s_add_u32 m0, %[j2], 90112\n\t" \
      "s_bfe_u32 s61, s71, 0x5000f\n\t" \
      "s_mul_i32 s61, s61, 8192\n\t" \
      "buffer_load_dwordx4 %[ya], %[ws], s61 offen offset:0 sc1 lds\n\t" \
      "buffer_load_dwordx4 %[ya], %[ws], s61 offen offset:1024 sc1 lds\n\t" \
      "s_add_u32 m0, s68, 73728\n\t" \
      "s_bfe_u32 s60, s71, 0x5000f\n\t" \
      "s_mul_i32 s60, s60, 262144\n\t" \
      "buffer_load_dwordx4 %[yb0], %[w2], s60 offen offset:0 sc1 lds\n\t" \
      "buffer_load_dwordx4 %[yb1], %[w2], s60 offen offset:1024 sc1 lds\n\t" \
      "s_waitcnt vmcnt(16)\n\t" \
      "s_barrier\n\t" \
      "buffer_load_dwordx4 %[yb2], %[w2], s60 offen offset:2048 sc1 lds\n\t" \
      "buffer_load_dwordx4 %[yb3], %[w2], s60 offen offset:3072 sc1 lds\n\t" \
      "s_add_u32 m0, %[j2], 114688\n\t" \
      "s_bfe_u32 s61, s71, 0x50014\n\t" \
      "s_mul_i32 s61, s61, 8192\n\t" \
      "buffer_load_dwordx4 %[ya], %[ws], s61 offen offset:0 sc1 lds\n\t" \
      "buffer_load_dwordx4 %[ya], %[ws], s61 offen offset:1024 sc1 lds\n\t" \
      "s_add_u32 m0, s68, 98304\n\t" \
      "s_bfe_u32 s60, s71, 0x50014\n\t" \
      "s_mul_i32 s60, s60, 262144\n\t" \
      "buffer_load_dwordx4 %[yb0], %[w2], s60 offen offset:0 sc1 lds\n\t" \
      "buffer_load_dwordx4 %[yb1], %[w2], s60 offen offset:1024 sc1 lds\n\t" \
      "s_waitcnt vmcnt(16)\n\t" \
      "s_barrier\n\t" \
      "buffer_load_dwordx4 %[yb2], %[w2], s60 offen offset:2048 sc1 lds\n\t" \
      "buffer_load_dwordx4 %[yb3], %[w2], s60 offen offset:3072 sc1 lds\n\t" \
      "s_add_u32 m0, %[j2], 139264\n\t" \
      "s_bfe_u32 s61, s71, 0x50019\n\t" \
      "s_mul_i32 s61, s61, 8192\n\t" \
      "buffer_load_dwordx4 %[ya], %[ws], s61 offen offset:0 sc1 lds\n\t" \
      "buffer_load_dwordx4 %[ya], %[ws], s61 offen offset:1024 sc1 lds\n\t" \
      "s_add_u32 m0, s68, 122880\n\t" \
      "s_bfe_u32 s60, s71, 0x50019\n\t" \
      "s_mul_i32 s60, s60, 262144\n\t" \
      "buffer_load_dwordx4 %[yb0], %[w2], s60 offen offset:0 sc1 lds\n\t" \
      "buffer_load_dwordx4 %[yb1], %[w2], s60 offen offset:1024 sc1 lds\n\t" \
      "s_waitcnt vmcnt(16)\n\t" \
      "s_barrier\n\t" \
      "buffer_load_dwordx4 %[yb2], %[w2], s60 offen offset:2048 sc1 lds\n\t" \
      "buffer_load_dwordx4 %[yb3], %[w2], s60 offen offset:3072 sc1 lds\n\t" \
      "s_add_u32 m0, %[j2], 16384\n\t" \
      "s_bfe_u32 s61, s72, 0x50000\n\t" \
      "s_mul_i32 s61, s61, 8192\n\t" \
      "buffer_load_dwordx4 %[ya], %[ws], s61 offen offset:0 sc1 lds\n\t" \
      "buffer_load_dwordx4 %[ya], %[ws], s61 offen offset:1024 sc1 lds\n\t" \
      "s_add_u32 m0, s68, 0\n\t" \
      "s_bfe_u32 s60, s72, 0x50000\n\t" \
      "s_mul_i32 s60, s60, 262144\n\t" \
      "buffer_load_dwordx4 %[yb0], %[w2], s60 offen offset:0 sc1 lds\n\t" \
      "buffer_load_dwordx4 %[yb1], %[w2], s60 offen offset:1024 sc1 lds\n\t" \
      "s_waitcnt vmcnt(16)\n\t" \
      "s_barrier\n\t" \
      "buffer_load_dwordx4 %[yb2], %[w2], s60 offen offset:2048 sc1 lds\n\t" \
      "buffer_load_dwordx4 %[yb3], %[w2], s60 offen offset:3072 sc1 lds\n\t" \
      "s_add_u32 m0, %[j2], 40960\n\t" \
      "s_bfe_u32 s61, s72, 0x50005\n\t" \
      "s_mul_i32 s61, s61, 8192\n\t" \
      "buffer_load_dwordx4 %[ya], %[ws], s61 offen offset:0 sc1 lds\n\t" \
      "buffer_load_dwordx4 %[ya], %[ws], s61 offen offset:1024 sc1 lds\n\t" \
      "s_add_u32 m0, s68, 24576\n\t" \
      "s_bfe_u32 s60, s72, 0x50005\n\t" \
      "s_mul_i32 s60, s60, 262144\n\t" \
      "buffer_load_dwordx4 %[yb0], %[w2], s60 offen offset:0 sc1 lds\n\t" \
      "s_waitcnt vmcnt(15)\n\t" \
      "s_barrier\n\t" \
      "buffer_load_dwordx4 %[yb1], %[w2], s60 offen offset:1024 sc1 lds\n\t" \
      "buffer_load_dwordx4 %[yb2], %[w2], s60 offen offset:2048 sc1 lds\n\t" \
      "buffer_load_dwordx4 %[yb3], %[w2], s60 offen offset:3072 sc1 lds\n\t" \
      "s_add_u32 m0, %[j2], 65536\n\t" \
      "s_bfe_u32 s61, s72, 0x5000a\n\t" \
      "s_mul_i32 s61, s61, 8192\n\t" \
      "buffer_load_dwordx4 %[ya], %[ws], s61 offen offset:0 sc1 lds\n\t" \
      "buffer_load_dwordx4 %[ya], %[ws], s61 offen offset:1024 sc1 lds\n\t" \
      "s_add_u32 m0, s68, 49152\n\t" \
      "s_bfe_u32 s60, s72, 0x5000a\n\t" \
      "s_mul_i32 s60, s60, 262144\n\t" \
      "buffer_load_dwordx4 %[yb0], %[w2], s60 offen offset:0 sc1 lds\n\t" \
      "s_waitcnt vmcnt(15)\n\t" \
      "s_barrier\n\t" \
      "buffer_load_dwordx4 %[yb1], %[w2], s60 offen offset:1024 sc1 lds\n\t" \
      "buffer_load_dwordx4 %[yb2], %[w2], s60 offen offset:2048 sc1 lds\n\t" \
      "buffer_load_dwordx4 %[yb3], %[w2], s60 offen offset:3072 sc1 lds\n\t" \
      "s_add_u32 m0, %[j2], 90112\n\t" \
      "s_bfe_u32 s61, s72, 0x5000f\n\t" \
      "s_mul_i32 s61, s61, 8192\n\t" \
      "buffer_load_dwordx4 %[ya], %[ws], s61 offen offset:0 sc1 lds\n\t" \
      "buffer_load_dwordx4 %[ya], %[ws], s61 offen offset:1024 sc1 lds\n\t" \
      "s_add_u32 m0, s68, 73728\n\t" \
      "s_bfe_u32 s60, s72, 0x5000f\n\t" \
      "s_mul_i32 s60, s60, 262144\n\t" \
      "buffer_load_dwordx4 %[yb0], %[w2], s60 offen offset:0 sc1 lds\n\t" \
      "s_waitcnt vmcnt(15)\n\t" \
      "s_barrier\n\t" \
      "buffer_load_dword %[fv], %[f4], %[ws], 0 offen sc1\n\t" \
      "buffer_load_dwordx4 %[yb1], %[w2], s60 offen offset:1024 sc1 lds\n\t" \
      "buffer_load_dwordx4 %[yb2], %[w2], s60 offen offset:2048 sc1 lds\n\t" \
      "buffer_load_dwordx4 %[yb3], %[w2], s60 offen offset:3072 sc1 lds\n\t" \
      "s_add_u32 m0, %[j2], 114688\n\t" \
      "s_bfe_u32 s61, s72, 0x50014\n\t" \
      "s_mul_i32 s61, s61, 8192\n\t" \
      "buffer_load_dwordx4 %[ya], %[ws], s61 offen offset:0 sc1 lds\n\t" \
      "buffer_load_dwordx4 %[ya], %[ws], s61 offen offset:1024 sc1 lds\n\t" \
      "s_add_u32 m0, s68, 98304\n\t" \
      "s_bfe_u32 s60, s72, 0x50014\n\t" \
      "s_mul_i32 s60, s60, 262144\n\t" \
      "buffer_load_dwordx4 %[yb0], %[w2], s60 offen offset:0 sc1 lds\n\t" \
      "s_waitcnt vmcnt(16)\n\t" \
      "s_barrier\n\t" \
      "buffer_load_dwordx4 %[yb1], %[w2], s60 offen offset:1024 sc1 lds\n\t" \
      "buffer_load_dwordx4 %[yb2], %[w2], s60 offen offset:2048 sc1 lds\n\t" \
      "buffer_load_dwordx4 %[yb3], %[w2], s60 offen offset:3072 sc1 lds\n\t" \
      "s_add_u32 m0, %[j2], 139264\n\t" \
      "s_bfe_u32 s61, s72, 0x50019\n\t" \
      "s_mul_i32 s61, s61, 81920\n\t" \
      "buffer_load_dwordx4 %[za0], %[wo], s61 offen offset:0 sc1 lds\n\t" \
      "buffer_load_dwordx4 %[za1], %[wo], s61 offen offset:1024 sc1 lds\n\t" \
      "s_waitcnt vmcnt(15)\n\t" \
      "s_barrier\n\t" \
      "s_waitcnt vmcnt(11)\n\t" \
      "12:\n\t" \
      "v_cmp_ne_u32 vcc, %[tag], %[fv]\n\t" \
      "s_add_u32 s62, s62, 1\n\t" \
      "s_cbranch_vccz 112f\n\t" \
      "s_cmp_gt_u32 s62, 0x20000\n\t" \
      "s_cbranch_scc1 112f\n\t" \
      "13:\n\t" \
      "buffer_load_dword %[fv], %[f4], %[ws], 0 offen sc1\n\t" \
      "s_waitcnt vmcnt(0)\n\t" \
      "s_branch 12b\n\t" \
      "112:\n\t" \
      "s_add_u32 m0, s68, 122880\n\t" \
      "s_bfe_u32 s60, s72, 0x50019\n\t" \
      "s_mul_i32 s60, s60, 262144\n\t" \
      "buffer_load_dwordx4 %[zb0], %[ws], s60 offen offset:0 sc1 lds\n\t" \
      "buffer_load_dwordx4 %[zb1], %[ws], s60 offen offset:1024 sc1 lds\n\t" \
      "buffer_load_dwordx4 %[zb2], %[ws], s60 offen offset:2048 sc1 lds\n\t" \
      "buffer_load_dwordx4 %[zb3], %[ws], s60 offen offset:3072 sc1 lds\n\t" \
      "s_add_u32 m0, %[j2], 16384\n\t" \
      "s_bfe_u32 s61, s73, 0x50000\n\t" \
      "s_mul_i32 s61, s61, 81920\n\t" \
      "buffer_load_dwordx4 %[za0], %[wo], s61 offen offset:0 sc1 lds\n\t" \
      "buffer_load_dwordx4 %[za1], %[wo], s61 offen offset:1024 sc1 lds\n\t" \
      "s_waitcnt vmcnt(14)\n\t" \
      "s_barrier\n\t" \
      "s_add_u32 m0, s68, 0\n\t" \
      "s_bfe_u32 s60, s73, 0x50000\n\t" \
      "s_mul_i32 s60, s60, 262144\n\t" \
      "buffer_load_dwordx4 %[zb0], %[ws], s60 offen offset:0 sc1 lds\n\t" \
      "buffer_load_dwordx4 %[zb1], %[ws], s60 offen offset:1024 sc1 lds\n\t" \
      "buffer_load_dwordx4 %[zb2], %[ws], s60 offen offset:2048 sc1 lds\n\t" \
      "buffer_load_dwordx4 %[zb3], %[ws], s60 offen offset:3072 sc1 lds\n\t" \
      "s_add_u32 m0, %[j2], 40960\n\t" \
      "s_bfe_u32 s61, s73, 0x50005\n\t" \
      "s_mul_i32 s61, s61, 81920\n\t" \
      "buffer_load_dwordx4 %[za0], %[wo], s61 offen offset:0 sc1 lds\n\t" \
      "buffer_load_dwordx4 %[za1], %[wo], s61 offen offset:1024 sc1 lds\n\t" \
      "s_waitcnt vmcnt(14)\n\t" \
      "s_barrier\n\t" \
      "s_add_u32 m0, s68, 24576\n\t" \
      "s_bfe_u32 s60, s73, 0x50005\n\t" \
      "s_mul_i32 s60, s60, 262144\n\t" \
      "buffer_load_dwordx4 %[zb0], %[ws], s60 offen offset:0 sc1 lds\n\t" \
      "buffer_load_dwordx4 %[zb1], %[ws], s60 offen offset:1024 sc1 lds\n\t" \
      "buffer_load_dwordx4 %[zb2], %[ws], s60 offen offset:2048 sc1 lds\n\t" \
      "buffer_load_dwordx4 %[zb3], %[ws], s60 offen offset:3072 sc1 lds\n\t" \
      "s_add_u32 m0, %[j2], 65536\n\t" \
      "s_bfe_u32 s61, s73, 0x5000a\n\t" \
      "s_mul_i32 s61, s61, 81920\n\t" \
      "buffer_load_dwordx4 %[za0], %[wo], s61 offen offset:0 sc1 lds\n\t" \
      "s_waitcnt vmcnt(13)\n\t" \
      "s_barrier\n\t" \
      "buffer_load_dwordx4 %[za1], %[wo], s61 offen offset:1024 sc1 lds\n\t" \
      "s_add_u32 m0, s68, 49152\n\t" \
      "s_bfe_u32 s60, s73, 0x5000a\n\t" \
      "s_mul_i32 s60, s60, 262144\n\t" \
      "buffer_load_dwordx4 %[zb0], %[ws], s60 offen offset:0 sc1 lds\n\t" \
      "buffer_load_dwordx4 %[zb1], %[ws], s60 offen offset:1024 sc1 lds\n\t" \
      "buffer_load_dwordx4 %[zb2], %[ws], s60 offen offset:2048 sc1 lds\n\t" \
      "buffer_load_dwordx4 %[zb3], %[ws], s60 offen offset:3072 sc1 lds\n\t" \
      "s_add_u32 m0, %[j2], 90112\n\t" \
      "s_bfe_u32 s61, s73, 0x5000f\n\t" \
      "s_mul_i32 s61, s61, 81920\n\t" \
      "buffer_load_dwordx4 %[za0], %[wo], s61 offen offset:0 sc1 lds\n\t" \
      "s_waitcnt vmcnt(13)\n\t" \
      "s_barrier\n\t" \
      "buffer_load_dwordx4 %[za1], %[wo], s61 offen offset:1024 sc1 lds\n\t" \
      "s_add_u32 m0, s68, 73728\n\t" \
      "s_bfe_u32 s60, s73, 0x5000f\n\t" \
      "s_mul_i32 s60, s60, 262144\n\t" \
      "buffer_load_dwordx4 %[zb0], %[ws], s60 offen offset:0 sc1 lds\n\t" \
      "buffer_load_dwordx4 %[zb1], %[ws], s60 offen offset:1024 sc1 lds\n\t" \
      "buffer_load_dwordx4 %[zb2], %[ws], s60 offen offset:2048 sc1 lds\n\t" \
      "buffer_load_dwordx4 %[zb3], %[ws], s60 offen offset:3072 sc1 lds\n\t" \
      "s_add_u32 m0, %[j2], 114688\n\t" \
      "s_bfe_u32 s61, s73, 0x50014\n\t" \
      "s_mul_i32 s61, s61, 81920\n\t" \
      "buffer_load_dwordx4 %[za0], %[wo], s61 offen offset:0 sc1 lds\n\t" \
      "s_waitcnt vmcnt(13)\n\t" \
      "s_barrier\n\t" \
      "buffer_load_dwordx4 %[za1], %[wo], s61 offen offset:1024 sc1 lds\n\t" \
      "s_add_u32 m0, s68, 98304\n\t" \
      "s_bfe_u32 s60, s73, 0x50014\n\t" \
      "s_mul_i32 s60, s60, 262144\n\t" \
      "buffer_load_dwordx4 %[zb0], %[ws], s60 offen offset:0 sc1 lds\n\t" \
      "buffer_load_dwordx4 %[zb1], %[ws], s60 offen offset:1024 sc1 lds\n\t" \
      "buffer_load_dwordx4 %[zb2], %[ws], s60 offen offset:2048 sc1 lds\n\t" \
      "buffer_load_dwordx4 %[zb3], %[ws], s60 offen offset:3072 sc1 lds\n\t" \
      "s_add_u32 m0, %[j2], 139264\n\t" \
      "s_bfe_u32 s61, s73, 0x50019\n\t" \
      "s_mul_i32 s61, s61, 81920\n\t" \
      "buffer_load_dwordx4 %[za0], %[wo], s61 offen offset:0 sc1 lds\n\t" \
      "s_waitcnt vmcnt(13)\n\t" \
      "s_barrier\n\t" \
      "buffer_load_dwordx4 %[za1], %[wo], s61 offen offset:1024 sc1 lds\n\t" \
      "s_add_u32 m0, s68, 122880\n\t" \
      "s_bfe_u32 s60, s73, 0x50019\n\t" \
      "s_mul_i32 s60, s60, 262144\n\t" \
      "buffer_load_dwordx4 %[zb0], %[ws], s60 offen offset:0 sc1 lds\n\t" \
      "buffer_load_dwordx4 %[zb1], %[ws], s60 offen offset:1024 sc1 lds\n\t" \
      "buffer_load_dwordx4 %[zb2], %[ws], s60 offen offset:2048 sc1 lds\n\t" \
      "buffer_load_dwordx4 %[zb3], %[ws], s60 offen offset:3072 sc1 lds\n\t" \
      "s_waitcnt vmcnt(12)\n\t" \
      "s_barrier\n\t" \
      "buffer_load_dword %[fv], %[f5], %[ws], 0 offen sc1\n\t" \
      "s_add_u32 m0, %[j2], 16384\n\t" \
      "s_bfe_u32 s61, s74, 0x50000\n\t" \
      "s_mul_i32 s61, s61, 81920\n\t" \
      "buffer_load_dwordx4 %[za0], %[wo], s61 offen offset:0 sc1 lds\n\t" \
      "buffer_load_dwordx4 %[za1], %[wo], s61 offen offset:1024 sc1 lds\n\t" \
      "s_add_u32 m0, s68, 0\n\t" \
      "s_bfe_u32 s60, s74, 0x50000\n\t" \
      "s_mul_i32 s60, s60, 262144\n\t" \
      "buffer_load_dwordx4 %[zb0], %[ws], s60 offen offset:0 sc1 lds\n\t" \
      "buffer_load_dwordx4 %[zb1], %[ws], s60 offen offset:1024 sc1 lds\n\t" \
      "buffer_load_dwordx4 %[zb2], %[ws], s60 offen offset:2048 sc1 lds\n\t" \
      "buffer_load_dwordx4 %[zb3], %[ws], s60 offen offset:3072 sc1 lds\n\t" \
      "s_waitcnt vmcnt(13)\n\t" \
      "s_barrier\n\t" \
      "s_add_u32 m0, %[j2], 40960\n\t" \
      "s_bfe_u32 s61, s74, 0x50005\n\t" \
      "s_mul_i32 s61, s61, 81920\n\t" \
      "buffer_load_dwordx4 %[za0], %[wo], s61 offen offset:0 sc1 lds\n\t" \
      "buffer_load_dwordx4 %[za1], %[wo], s61 offen offset:1024 sc1 lds\n\t" \
      "s_add_u32 m0, s68, 24576\n\t" \
      "s_bfe_u32 s60, s74, 0x50005\n\t" \
      "s_mul_i32 s60, s60, 262144\n\t" \
      "buffer_load_dwordx4 %[zb0], %[ws], s60 offen offset:0 sc1 lds\n\t" \
      "buffer_load_dwordx4 %[zb1], %[ws], s60 offen offset:1024 sc1 lds\n\t" \
      "buffer_load_dwordx4 %[zb2], %[ws], s60 offen offset:2048 sc1 lds\n\t" \
      "buffer_load_dwordx4 %[zb3], %[ws], s60 offen offset:3072 sc1 lds\n\t" \
      "s_waitcnt vmcnt(13)\n\t" \
      "s_barrier\n\t" \
      "s_add_u32 m0, %[j2], 65536\n\t" \
      "s_bfe_u32 s61, s74, 0x5000a\n\t" \
      "s_mul_i32 s61, s61, 81920\n\t" \
      "buffer_load_dwordx4 %[za0], %[wo], s61 offen offset:0 sc1 lds\n\t" \
      "buffer_load_dwordx4 %[za1], %[wo], s61 offen offset:1024 sc1 lds\n\t" \
      "s_add_u32 m0, s68, 49152\n\t" \
      "s_bfe_u32 s60, s74, 0x5000a\n\t" \
      "s_mul_i32 s60, s60, 262144\n\t" \
      "buffer_load_dwordx4 %[zb0], %[ws], s60 offen offset:0 sc1 lds\n\t" \
      "buffer_load_dwordx4 %[zb1], %[ws], s60 offen offset:1024 sc1 lds\n\t" \
      "buffer_load_dwordx4 %[zb2], %[ws], s60 offen offset:2048 sc1 lds\n\t" \
      "buffer_load_dwordx4 %[zb3], %[ws], s60 offen offset:3072 sc1 lds\n\t" \
      "s_waitcnt vmcnt(18)\n\t" \
      "v_cmp_ne_u32 vcc, %[tag], %[fv]\n\t" \
      "s_nop 0\n\t" \
      "s_mov_b64 s[66:67], vcc\n\t" \
      "s_waitcnt vmcnt(12)\n\t" \
      "s_barrier\n\t" \
      "s_add_u32 m0, s68, 73728\n\t" \
      "s_cmp_eq_u64 s[66:67], 0\n\t" \
      "s_cbranch_scc0 3f\n\t" \
      "buffer_load_dwordx4 %[yo], %[ws], 0 offen offset:0 sc1 lds\n\t" \
      "buffer_load_dwordx4 %[yo], %[ws], 0 offen offset:1024 sc1 lds\n\t" \
      "buffer_load_dwordx4 %[yo], %[ws], s63 offen offset:2048 sc1 lds\n\t" \
      "buffer_load_dwordx4 %[yo], %[ws], s63 offen offset:3072 sc1 lds\n\t" \
      "s_branch 5f\n\t" \
      "3:\n\t" \
      "buffer_load_dword %[fv], %[f5], %[ws], 0 offen sc1\n\t" \
      "buffer_load_dword %[fv], %[f5], %[ws], 0 offen sc1\n\t" \
      "buffer_load_dword %[fv], %[f5], %[ws], 0 offen sc1\n\t" \
      "buffer_load_dword %[fv], %[f5], %[ws], 0 offen sc1\n\t" \
      "5:\n\t" \
      "s_waitcnt vmcnt(10)\n\t" \
      "s_barrier\n\t" \
      "s_waitcnt vmcnt(4)\n\t" \
      "s_barrier\n\t" \
      "s_cmp_eq_u64 s[66:67], 0\n\t" \
      "s_cbranch_scc1 4f\n\t" \
      "s_branch 15f\n\t" \
      "14:\n\t" \
      "v_cmp_ne_u32 vcc, %[tag], %[fv]\n\t" \
      "s_add_u32 s62, s62, 1\n\t" \
      "s_cbranch_vccz 114f\n\t" \
      "s_cmp_gt_u32 s62, 0x20000\n\t" \
      "s_cbranch_scc1 114f\n\t" \
      "15:\n\t" \
      "buffer_load_dword %[fv], %[f5], %[ws], 0 offen sc1\n\t" \
      "s_waitcnt vmcnt(0)\n\t" \
      "s_branch 14b\n\t" \
      "114:\n\t" \
      "buffer_load_dwordx4 %[yo], %[ws], 0 offen offset:0 sc1 lds\n\t" \
      "buffer_load_dwordx4 %[yo], %[ws], 0 offen offset:1024 sc1 lds\n\t" \
      "buffer_load_dwordx4 %[yo], %[ws], s63 offen offset:2048 sc1 lds\n\t" \
      "buffer_load_dwordx4 %[yo], %[ws], s63 offen offset:3072 sc1 lds\n\t" \
      "4:\n\t" \
      "s_waitcnt vmcnt(0)\n\t" \
      "s_barrier\n\t"
#define TL_ASM \
      "s_waitcnt vmcnt(9)\n\t" \
      "s_barrier\n\t" \
      "s_waitcnt vmcnt(0)\n\t" \
      "s_barrier\n\t" \
      "s_barrier\n\t" \
      "s_barrier\n\t"
#define QORD0 0x9768532410ull
#define QORD1 0x8796435210ull
#define QORD2 0x9875463102ull
#define QORD3 0x9865742130ull
#define S_CLOB "s60", "s61", "s62", "s63", "s64", "s65", "s66", "s67", "s68", "s69", "s70", "s71", "s72", "s73", "s74"

#define LD(v, r, s, o) "buffer_load_dwordx4 " v ", " r ", " s " offen offset:" #o " sc1 lds\n\t"
template <int NA, int W, int PF = 0>
DI void stream(const unsigned (&va)[5], const unsigned (&vb)[4], rsrc_t ra, rsrc_t rb, unsigned tsa, unsigned tsb, unsigned tls, unsigned da,
               unsigned i, unsigned iend, unsigned isync, unsigned& tl, rsrc_t rp, unsigned& po, unsigned cp = 0, unsigned cq = 0) {
  asm volatile(
      ".if %[PF] == 1\n\ts_buffer_load_dword %[po], %[rp], 0x0\n\t.endif\n\t"
      ".if %[PF] == 2\n\ts_mov_b32 s60, %[cp]\n\t.rept 5\n\ts_buffer_load_dword s63, %[rp], s60\n\ts_add_u32 s60, s60, %[cq]\n\t.endr\n\t.endif\n\t"
      "s_nop 0\n\tv_readlane_b32 s60, %[tsb], %[i]\n\tv_readlane_b32 s61, %[tsa], %[i]\n\tv_readlane_b32 s62, %[tls], %[i]\n\ts_nop 2\n\t"
      "1:\n\t"
      "s_mov_b32 m0, s62\n\t"
      "s_cmp_lt_u32 %[i], %[isync]\n\ts_cbranch_scc1 2f\n\ts_waitcnt vmcnt(%[W])\n\ts_barrier\n\t2:\n\t"
      LD("%[b0]", "%[rb]", "s60", 0) LD("%[b1]", "%[rb]", "s60", 1024) LD("%[b2]", "%[rb]", "s60", 2048) LD("%[b3]", "%[rb]", "s60", 3072)
      "s_add_u32 m0, s62, %[da]\n\ts_add_u32 %[i], %[i], 1\n\t"
      "v_readlane_b32 s60, %[tsb], %[i]\n\tv_readlane_b32 s62, %[tls], %[i]\n\t"
      LD("%[a0]", "%[ra]", "s61", 0) LD("%[a1]", "%[ra]", "s61", 1024)
      ".if %[NA] > 2\n\t" LD("%[a2]", "%[ra]", "s61", 2048) LD("%[a3]", "%[ra]", "s61", 3072) ".endif\n\t"
      ".if %[NA] > 4\n\ts_add_u32 m0, m0, 4096\n\ts_nop 0\n\t" LD("%[a4]", "%[ra]", "s61", 0) ".endif\n\t"
      "v_readlane_b32 s61, %[tsa], %[i]\n\t"
      "s_cmp_lt_u32 %[i], %[iend]\n\ts_cbranch_scc1 1b\n\t"
      ".if %[PF]\n\ts_waitcnt lgkmcnt(0)\n\t.endif"
      : [i] "+s"(i), [po] "=&s"(po)
      : [a0] "v"(va[0]), [a1] "v"(va[1]), [a2] "v"(va[NA > 2 ? 2 : 0]), [a3] "v"(va[NA > 2 ? 3 : 0]), [a4] "v"(va[NA > 4 ? 4 : 0]), [b0] "v"(vb[0]),
        [b1] "v"(vb[1]), [b2] "v"(vb[2]), [b3] "v"(vb[3]), [ra] "s"(ra), [rb] "s"(rb), [tsa] "v"(tsa), [tsb] "v"(tsb), [tls] "v"(tls), [da] "s"(da),
        [iend] "s"(iend), [isync] "s"(isync), [W] "i"(W), [NA] "i"(NA), [PF] "i"(PF), [rp] "s"(rp), [cp] "s"(cp), [cq] "s"(cq)
      : "memory", "scc", "s60", "s61", "s62", "s63");
}
template <int N>
DI void issue(const unsigned* v, rsrc_t r, unsigned so, unsigned lds) {
  asm volatile("s_mov_b32 m0, %7\n\ts_nop 3\n\t" LD("%0", "%5", "%6", 0) LD("%1", "%5", "%6", 1024)
               ".if %8 > 2\n\t" LD("%2", "%5", "%6", 2048) LD("%3", "%5", "%6", 3072) ".endif\n\t"
               ".if %8 > 4\n\ts_add_u32 m0, m0, 4096\n\ts_nop 0\n\t" LD("%4", "%5", "%6", 0) ".endif"
               ::"v"(v[0]), "v"(v[1]), "v"(v[N > 2 ? 2 : 0]), "v"(v[N > 2 ? 3 : 0]), "v"(v[N > 4 ? 4 : 0]), "s"(r), "s"(so), "s"(lds), "i"(N) : "memory", "scc");
}
DI void off256(unsigned (&v)[4], unsigned rs, unsigned colb, int j, int l) {
  const int g = l >> 4;
  v[0] = (16 * j + g) * rs + colb + (((l & 15) ^ (2 * g)) * 16);
#pragma unroll
  for (int i = 1; i < 4; ++i) v[i] = (v[0] ^ ((i >> 1) * 128)) + i * (4 * rs - 1024);
}
DI unsigned rd256(int nt, int l) {
  const int g = l >> 4, q = (l & 15) >> 2;
  return (8 * g + q) * 256 + ((nt ^ (q | ((g & 1) << 2))) * 32) + 8 * (l & 3);
}

constexpr unsigned LNRS = 1344;
#define LNC ((blockIdx.x & 7) * 32 + (blockIdx.x >> 3))
DI float half_sum(float v) {
  v += dpp<0xB1>(v);
  v += dpp<0x4E>(v);
  v += dpp<0x141>(v);
  v += dpp<0x140>(v);
  const unsigned u = __builtin_bit_cast(unsigned, v);
  auto a = __builtin_amdgcn_permlane16_swap(u, u, false, false);
  return __builtin_bit_cast(float, (unsigned)a[0]) + __builtin_bit_cast(float, (unsigned)a[1]);
}
DI void ln_rows(const u32x2 (&u)[5], unsigned first, unsigned lo, unsigned so, int l) {
  const float pv = bflo(first);
  f32x2 v[10], s = {0.f, 0.f}, q = {0.f, 0.f};
#pragma unroll
  for (int j = 0; j < 10; ++j) {
    v[j] = f32x2{bflo(u[j >> 1][j & 1]), bfhi(u[j >> 1][j & 1])} - pv;
    s += v[j]; q += v[j] * v[j];
  }
  const float m = half_sum(s[0] + s[1]) * (1.f / D);
  const float var = fmaxf(half_sum(q[0] + q[1]) * (1.f / D) - m * m, 0.f);
  const float r = __builtin_amdgcn_rsqf(var + 1e-5f);
  const float rh = r * __builtin_amdgcn_rsqf(var * r * r + 1e-5f), ch = -m * rh;
#pragma unroll
  for (int j = 0; j < 5; ++j) {
    const f32x2 a = v[2 * j] * rh + ch, b = v[2 * j + 1] * rh + ch;
    *(LAS u32x2*)(lo + j * 256) = u32x2{pk2(a[0], a[1]), pk2(b[0], b[1])};
  }
  *(LAS float*)so = (l & 1) ? -(m + pv) * r : r;
}
DI void ln_store(rsrc_t rw, unsigned ta, const unsigned (&ho)[3], int w, int l) {
  const bf16x8 c0 = trd2(ta, 4 * LNRS), c1 = trd2(ta + 512, 4 * LNRS);
  u32x4 c2;
  if (w < 2) c2 = __builtin_bit_cast(u32x4, trd2(ta + 1024, 4 * LNRS));
  else c2 = *(LAS u32x4*)(LNB + 8 * LNRS + (l & 3) * 16);
  __builtin_amdgcn_raw_buffer_store_b128(__builtin_bit_cast(u32x4, c0), rw, ho[0], 0, SC1);
  __builtin_amdgcn_raw_buffer_store_b128(__builtin_bit_cast(u32x4, c1), rw, ho[1], 0, SC1);
  if (w != 2) __builtin_amdgcn_raw_buffer_store_b128(c2, rw, ho[2], 0, SC1);
}
#define ACC16(A) [c0] "+v"(A[0][0]), [c1] "+v"(A[0][1]), [c2] "+v"(A[0][2]), [c3] "+v"(A[0][3]), [c4] "+v"(A[1][0]), [c5] "+v"(A[1][1]), \
    [c6] "+v"(A[1][2]), [c7] "+v"(A[1][3]), [c8] "+v"(A[2][0]), [c9] "+v"(A[2][1]), [c10] "+v"(A[2][2]), [c11] "+v"(A[2][3]),            \
    [c12] "+v"(A[3][0]), [c13] "+v"(A[3][1]), [c14] "+v"(A[3][2]), [c15] "+v"(A[3][3])
#define ACC8(A) [c0] "+v"(A[0][0]), [c1] "+v"(A[0][1]), [c2] "+v"(A[1][0]), [c3] "+v"(A[1][1]), [c4] "+v"(A[2][0]), [c5] "+v"(A[2][1]), \
    [c6] "+v"(A[3][0]), [c7] "+v"(A[3][1])

struct QPre { unsigned la, lb, fo, qo[4], so, co; };
DI void q_pre(QPre& q, int rp, int ct, int w, int l) {
  const int g = l >> 4, i16 = l & 15, mh = w >> 1, nh = w & 1;
  q.la = QB + 16384 + (64 * mh + 8 * (i16 >> 2) + (i16 & 3)) * 128 + ((g ^ (i16 >> 1)) * 16);
  q.lb = QB + rd256(4 * nh, l);
  q.fo = threadIdx.x == 0 ? F1_OFF + blockIdx.x * 64 : OOB;
#pragma unroll
  for (int n = 0; n < 4; ++n) {
    const int col = ct * 128 + 64 * nh + 16 * n + i16;
    q.qo[n] = QT_OFF + (col / 20) * 81920 + (rp * 16 + 8 * mh + g) * 320 + (col % 20) * 16;
    PIN(q.qo[n]);
  }
  q.so = SB + (8 * mh + g) * 64;
  q.co = SB + 1024 + (64 * nh + i16) * 8;
  PIN(q.la); PIN(q.lb); PIN(q.fo); PIN(q.so); PIN(q.co);
}
DI f32x4 phi4(f32x4 t, f32x4 kl, f32x4 kc, float eps) {
  const f32x4 a = t * kl + kc;
  f32x4 e, x;
#pragma unroll
  for (int r = 0; r < 4; ++r) { e[r] = __builtin_amdgcn_fmed3f(__builtin_amdgcn_exp2f(a[r]), 0.f, 1.f); x[r] = vmax(t[r], eps); }
  return x + e;
}
DI u32x4 pack8(f32x4 lo, f32x4 hi) { return u32x4{pk2(lo[0], lo[2]), pk2(hi[0], hi[2]), pk2(lo[1], lo[3]), pk2(hi[1], hi[3])}; }
struct ULd { unsigned a[5], b[4]; };
DI void u_ld(ULd& u, int ft, int dp, int j, int l) {
  u.a[0] = HT_OFF + dp * 327680 + j * 5120 + l * 16;
  u.a[4] = u.a[0] + 4096;
  u.b[0] = (32 * j + (l >> 3)) * 3456 + ft * 128 + (((l & 7) ^ ((l >> 3) & 2)) * 16);
#pragma unroll
  for (int i = 1; i < 4; ++i) u.b[i] = (u.b[0] ^ ((i & 1) * 64)) + i * (8 * 3456 - 1024);
  PIN(u.a[0]); PIN(u.a[4]); PIN(u.b[0]); PIN(u.b[1]); PIN(u.b[2]); PIN(u.b[3]);
  u.a[1] = u.a[2] = u.a[3] = u.a[0];
}
#define RL(v, k) __builtin_amdgcn_readlane(v, k)
DI void q_compute(const Params& p, rsrc_t rw, const QPre& q, f32x4 (&acc)[4][4], int ct, unsigned tag, int w, int l) {
  f32x16 r16, m16;
  u32x8 c8;
  {
    unsigned cur = 2 * 32768, cnt = 10;
    asm volatile(Q_LOOP : ACC16(acc), [cur] "+s"(cur), [cnt] "+s"(cnt), "=&{v[128:143]}"(r16), "=&{v[144:159]}"(m16), "=&{v[120:127]}"(c8)
                 : [la] "v"(q.la), [lb] "v"(q.lb), [hk] "i"(HK1), [tag] "v"(tag), [fo] "v"(q.fo), [rw] "s"(rw), [so] "v"(q.so), [co] "v"(q.co)
                 : "memory", "scc", Q_CLOB);
  }
  f32x4 rr[4], mr[4];
  float cs[4];
  bool ok = true;
#pragma unroll
  for (int n = 0; n < 4; ++n) {
    cs[n] = asf(c8[2 * n]); ok &= c8[2 * n + 1] == tag;
    rr[n] = f32x4{r16[4 * n], r16[4 * n + 1], r16[4 * n + 2], r16[4 * n + 3]};
    mr[n] = f32x4{m16[4 * n], m16[4 * n + 1], m16[4 * n + 2], m16[4 * n + 3]};
  }
  if (__builtin_expect(!__all(ok), 0)) {
#pragma unroll 1
    for (unsigned spins = 0; spins < (1u << 16); ++spins) {
      ok = true;
#pragma unroll
      for (int n = 0; n < 4; ++n) {
        const u32x2 v = __builtin_amdgcn_raw_buffer_load_b64(rw, CS_OFF + ct * 1024 + q.co - (SB + 1024) + n * 128, 0, SC1);
        cs[n] = asf(v[0]); ok &= v[1] == tag;
      }
      asm volatile("" ::: "memory");
      if (__all(ok)) break;
      __builtin_amdgcn_s_sleep(1);
    }
  }
  const bool phi = ct < 10;
  const float eps = phi ? 1e-6f : 0.f;
  const f32x4 kl = opaque4(1.44269504088896f), kc = opaque4(-1.44269504088896e-6f);
#pragma unroll
  for (int n = 0; n < 4; ++n) {
#pragma unroll
    for (int m = 0; m < 4; ++m) acc[m][n] = acc[m][n] * rr[m] + (mr[m] * cs[n] + eps);
    if (phi) {
#pragma unroll
      for (int m = 0; m < 4; ++m) acc[m][n] = phi4(acc[m][n], kl, kc, eps);
    }
    __builtin_amdgcn_raw_buffer_store_b128(pack8(acc[0][n], acc[1][n]), rw, q.qo[n], 0, SC1);
    __builtin_amdgcn_raw_buffer_store_b128(pack8(acc[2][n], acc[3][n]), rw, q.qo[n] + 1280, 0, SC1);
  }
}

constexpr unsigned XB = 40 * 1024;
struct UPre { unsigned la, lb, fo, fo3, ob[3], xw, xr; };
DI void u_pre(UPre& u, int ft, int dp, unsigned f2off, int w, int l) {
  const int g = l >> 4, i16 = l & 15, q = i16 >> 2, kh = w >> 1, nt = 2 * (w & 1) + kh;
  u.la = kh * 9216 + 4096 + g * 1280 + i16 * 16;
  u.lb = kh * 9216 + (8 * g + q) * 128 + ((nt ^ (((q >> 1) & 1) | ((g & 1) << 1))) * 32) + 8 * (l & 3);
  u.fo = threadIdx.x == 0 ? f2off : OOB;
  u.fo3 = threadIdx.x == 0 ? F3_OFF + (dp * 32 + ft) * 64 : OOB;
#pragma unroll
  for (int a = 0; a < 3; ++a) {
    const int t16 = dp * 5 + 2 * a + (g & 1);
    u.ob[a] = GP_OFF + (t16 >> 2) * 221184 + (ft * 8 + 2 * nt + (g >> 1)) * 1024 + (t16 & 3) * 256 + i16 * 16;
  }
  if (g & 1) u.ob[2] = OOB;
  u.xw = XB + w * 5120 + l * 16; u.xr = XB + (w ^ 2) * 5120 + l * 16;
  PIN(u.la); PIN(u.lb); PIN(u.fo); PIN(u.fo3); PIN(u.ob[0]); PIN(u.ob[1]); PIN(u.ob[2]); PIN(u.xw); PIN(u.xr);
}
DI void u_compute(const Params& p, rsrc_t rw, const UPre& u, u32x2 bb, unsigned tag, int w, int l) {
  f32x4 acc[5], oth[5];
#pragma unroll
  for (int i = 0; i < 5; ++i) { acc[i] = f32x4{bflo(bb[0]), bfhi(bb[0]), bflo(bb[1]), bfhi(bb[1])}; oth[i] = f32x4{0, 0, 0, 0}; }
  bar();
  {
    unsigned cur = 0, cnt = 15, tlc = 0;
    asm volatile(U_LOOP : [c0] "+v"(acc[0]), [c1] "+v"(oth[0]), [c2] "+v"(acc[1]), [c3] "+v"(oth[1]), [c4] "+v"(acc[2]), [c5] "+v"(oth[2]),
                   [c6] "+v"(acc[3]), [c7] "+v"(oth[3]), [c8] "+v"(acc[4]), [c9] "+v"(oth[4]), [cur] "+s"(cur), [cnt] "+s"(cnt)
                 : [la] "v"(u.la), [lb] "v"(u.lb), [hk] "i"(HK2 - 1), [tag] "v"(tag), [fo] "v"(u.fo), [rw] "s"(rw), [xw] "v"(u.xw), [xr] "v"(u.xr),
                   [k9] "s"((w >> 1) * 9216) : "memory", "scc", "s64", "s65", U_CLOB);
  }
  const f32x4 k3 = opaque4(-0.044715f * 2.302208198f), k1 = opaque4(-2.302208198f);
#pragma unroll
  for (int j = 0; j < 5; ++j) acc[j] += oth[j];
  SCHED();
  f32x4 e[5];
#pragma unroll
  for (int j = 0; j < 5; ++j) e[j] = acc[j] * acc[j];
  SCHED();
#pragma unroll
  for (int j = 0; j < 5; ++j) e[j] = e[j] * k3 + k1;
  SCHED();
#pragma unroll
  for (int j = 0; j < 5; ++j) { e[j] *= acc[j]; PIN(e[j]); }
  SCHED();
#pragma unroll
  for (int j = 0; j < 5; ++j)
#pragma unroll
    for (int r = 0; r < 4; ++r) e[j][r] = __builtin_amdgcn_exp2f(e[j][r]);
#pragma unroll
  for (int j = 0; j < 5; ++j) e[j] += 1.f;
  SCHED();
  unsigned o[5][2];
#pragma unroll
  for (int j = 0; j < 5; ++j) {
#pragma unroll
    for (int r = 0; r < 4; ++r) e[j][r] = __builtin_amdgcn_rcpf(e[j][r]);
    e[j] *= acc[j];
    o[j][0] = pk2(e[j][0], e[j][1]); o[j][1] = pk2(e[j][2], e[j][3]);
    if (j & 1) __builtin_amdgcn_raw_buffer_store_b128(swap16(o[j - 1][0], o[j - 1][1], o[j][0], o[j][1]), rw, u.ob[j >> 1], 0, SC1);
  }
  __builtin_amdgcn_raw_buffer_store_b128(swap16(o[4][0], o[4][1], o[4][0], o[4][1]), rw, u.ob[2], 0, SC1);
  bar_v<0>();
  __builtin_amdgcn_raw_buffer_store_b32(tag, rw, u.fo3, 0, SC1);
}

#define NHV 10
constexpr int NH = NHV;
constexpr int NT = 10 + 27 - NH;
constexpr unsigned YSLOT = 24576, YX = 6 * YSLOT, YA = (NT % 6) * YSLOT;
DI void y_compute(const Params& p, rsrc_t ro, const unsigned (&b2)[2], int st, int dt, int w, int l) {
  const int g = l >> 4, i16 = l & 15, q = i16 >> 2;
  unsigned xa = YX + (32 * w + i16) * 128 + (((g >> 1) ^ (i16 >> 1)) * 16) + (g & 1) * 8;
  f32x4 acc[4][2];
#pragma unroll
  for (int j = 0; j < 4; ++j)
#pragma unroll
    for (int n = 0; n < 2; ++n) {
      const u32x2 xv = *(LAS u32x2*)((xa ^ (j * 32)) + n * 2048);
      acc[j][n] = f32x4{bflo(xv[0]), bfhi(xv[0]), bflo(xv[1]), bfhi(xv[1])} + bflo(b2[n]);
      PIN(acc[j][n]);
    }
  unsigned la = 16384 + g * 1024 + i16 * 16, lb = rd256(2 * w, l);
  unsigned aa = YA + (g >> 1) * 2048 + (32 * w + i16) * 16 + (g & 1) * 8;
  unsigned oo = (st * 128 + 32 * w + (l >> 3)) * 1280 + dt * 128 + (((l & 7) ^ g) * 16);
  unsigned lc = 16384 + (8 * g + q) * 128 + ((((q >> 1) & 1) | ((g & 1) << 1)) * 32) + 8 * (l & 3);
  unsigned ld = 2 * g * 1024 + w * 256 + (g & 1) * 128 + i16 * 8;
  PIN(la); PIN(lb); PIN(lc); PIN(ld); PIN(xa); PIN(aa); PIN(oo);
  bar();
  u32x16 ao;
  {
    unsigned cur = 0, cnt = (NT - 11) / 2;
    asm volatile(Y_LOOP : ACC8(acc), [cur] "+s"(cur), [cnt] "+s"(cnt), "=&{v[144:159]}"(ao)
                 : [la] "v"(la), [lb] "v"(lb), [lc] "v"(lc), [ld] "v"(ld), [aa] "v"(aa) : "memory", "scc", Y_CLOB);
  }
#pragma unroll
  for (int j = 0; j < 4; ++j)
#pragma unroll
    for (int n = 0; n < 2; ++n) {
      const u32x2 av = {ao[4 * j + 2 * n], ao[4 * j + 2 * n + 1]};
      const f32x4 c = acc[j][n] + f32x4{bflo(av[0]), bfhi(av[0]), bflo(av[1]), bfhi(av[1])};
      *(LAS u32x2*)((xa ^ (j * 32)) + n * 2048) = u32x2{pk2(c[0], c[1]), pk2(c[2], c[3])};
    }
  asm volatile("s_waitcnt lgkmcnt(0)" ::: "memory");
#pragma unroll
  for (int i = 0; i < 4; ++i) {
    __builtin_amdgcn_raw_buffer_store_b128(*(LAS u32x4*)(YX + w * 4096 + i * 1024 + l * 16), ro, (oo ^ ((i & 1) * 64)) + i * 8 * 1280, 0, OAUX);
  }
}

DI void o_compute(const Params& p, rsrc_t rw, int st, int dt, unsigned tag, int w, int l) {
  const int g = l >> 4, i16 = l & 15, mh = w >> 1, nh = w & 1;
  const unsigned la = 16384 + mh * 8192 + g * 1024 + i16 * 16, lb = rd256(4 * nh, l);
  f32x4 acc[4][4];
#pragma unroll
  for (int m = 0; m < 4; ++m)
#pragma unroll
    for (int n = 0; n < 4; ++n) acc[m][n] = f32x4{0, 0, 0, 0};
  bar();
  {
    unsigned cur = 0, cnt = NH;
    asm volatile(H_LOOP : ACC16(acc), [cur] "+s"(cur), [cnt] "+s"(cnt) : [la] "v"(la), [lb] "v"(lb) : "memory", "scc", L_CLOB);
  }
  const unsigned ob = AO_OFF + (dt * 16 + 8 * mh + (g >> 1)) * 32768 + (st * 128 + 64 * nh + 16 * (g & 1) + i16) * 16;
#pragma unroll
  for (int P = 0; P < 2; ++P)
#pragma unroll
    for (int m = 0; m < 4; ++m) {
      const f32x4 a = acc[m][2 * P], b = acc[m][2 * P + 1];
      __builtin_amdgcn_raw_buffer_store_b128(swap16(pk2(a[0], a[1]), pk2(a[2], a[3]), pk2(b[0], b[1]), pk2(b[2], b[3])), rw, ob + P * 512 + m * 65536, 0, SC1);
    }
  bar_v<0>();
  if (w == 0) flag(rw, F5_OFF + (st * 5 + dt) * 64, tag, l);
}

DI unsigned attn(const Params& p, rsrc_t rw, int h, unsigned tag, int w, int l) {
  const int nrg = h < 16 ? 3 : 4, rg0 = nrg * w;
  const int g = l >> 4, i16 = l & 15, c32 = l & 31, hf = l >> 5;
  constexpr unsigned PW = 20480, KI = 13312, KP = 16384;
  unsigned ka = hf * 1024 + c32 * 8, kb = 2048 + l * 8;
  unsigned tq = w * PW + (4 * (g & 1) + (l & 3)) * 320 + (i16 >> 2) * 16;
  unsigned oa = OC_OFF + (5 * h + hf) * 16384 + rg0 * 512 + c32 * 16, pv = QT_OFF + h * 81920 + rg0 * 2560 + l * 16;
  unsigned fo = l < nrg ? F4_OFF + ((rg0 + l) * 32 + h) * 64 : l == 4 && h >= 16 ? F4_OFF + ((24 + w) * 32 + h - 16) * 64 : OOB;
  PIN(ka); PIN(kb); PIN(tq); PIN(oa); PIN(pv); PIN(fo);
  u32x2 a[6];
  {
    unsigned vo = QT_OFF + (32 + h) * 81920 + w * 10240 + l * 16, kr = w * PW + hf * 320 + c32 * 16;
    const bool one = c32 == 20 || c32 == 24;
    PIN(vo); PIN(kr); PIN(pv);
    if (w == 4) {
      unsigned o[2];
#pragma unroll
      for (int i = 0; i < 2; ++i) { const int sel = g + 4 * i; o[i] = F2_OFF + (((sel < 6 ? 5 * (sel >> 1) : 0) + ((20 * (sel < 6 ? h : h & 15) + 19 * (sel & 1)) >> 7)) * 16 + i16) * 64; }
      poll<2, 2>(rw, o, tag);
    }
    bar();
    const unsigned v5[5] = {vo, vo, vo, vo, vo + 4096};
    __builtin_amdgcn_s_setprio(1);
#pragma unroll 1
    for (int i = 0; i < 4; ++i) issue<5>(v5, rw, (i & 1) * (32 * 81920) + (i >> 1) * 5120, w * PW + i * 5120);
    __builtin_amdgcn_s_setprio(0);
    f32x16 kv;
#pragma unroll
    for (int r = 0; r < 16; ++r) kv[r] = 0.f;
    const u32x4 ones = {0x3f803f80u, 0x3f803f80u, 0x3f803f80u, 0x3f803f80u};
    wait_v<10>();
#pragma unroll 1
    for (int hh = 0; hh < 2; ++hh) {
      u32x4 kf[8], vf[8];
#pragma unroll
      for (int j = 0; j < 8; ++j) { kf[j] = *(LAS u32x4*)(kr + hh * 10240 + j * 640); vf[j] = *(LAS u32x4*)(kr + hh * 10240 + 5120 + j * 640); }
#pragma unroll
      for (int j = 0; j < 8; ++j)
        kv = __builtin_amdgcn_mfma_f32_32x32x16_bf16(__builtin_bit_cast(bf16x8, kf[j]), __builtin_bit_cast(bf16x8, one ? ones : vf[j]), kv, 0, 0, 0);
      asm volatile("s_waitcnt lgkmcnt(0)" ::: "memory");
      if (hh == 0) {
        const unsigned p5[5] = {pv, pv, pv, pv, pv + 4096};
        issue<5>(p5, rw, 0, w * PW);
        if (h < 16) { issue<4>(p5, rw, 5120, w * PW + 5120); wait_v<9>(); }
        else { issue<5>(p5, rw, 5120, w * PW + 5120); wait_v<10>(); }
      }
    }
    if (h >= 16) {
      const unsigned pf = QT_OFF + (h - 16) * 81920 + (24 + w) * 2560 + l * 16;
#pragma unroll 1
      for (int i = 0; i < 3; ++i) dma_sc1(pf, rw, i * 1024, w * PW + 10240 + i * 1024);
    }
#pragma unroll
    for (int i = 0; i < 3; ++i) *(LAS f32x4*)(w * PW + KP + i * 1024 + l * 16) = f32x4{kv[4 * i], kv[4 * i + 1], kv[4 * i + 2], kv[4 * i + 3]};
    bar_l();
    if (w < 3) {
      f32x4 s[8];
#pragma unroll
      for (int j = 0; j < 8; ++j) s[j] = *(LAS f32x4*)(j * PW + KP + w * 1024 + l * 16);
      const f32x4 t = ((s[0] + s[1]) + (s[2] + s[3])) + ((s[4] + s[5]) + (s[6] + s[7]));
      const u32x2 g0 = {pk2(t[0], t[1]), tag}, g1 = {pk2(t[2], t[3]), tag};
      const unsigned o = w * 1024 + l * 8;
      *(LAS u32x2*)(KI + o) = g0;
      *(LAS u32x2*)(KI + o + 512) = g1;
      __builtin_amdgcn_raw_buffer_store_b64(g0, rw, KV_OFF + h * 3072 + o, 0, SC1);
      __builtin_amdgcn_raw_buffer_store_b64(g1, rw, KV_OFF + h * 3072 + o + 512, 0, SC1);
      wait_v<2>();
    } else {
      wait_v<0>();
    }
    bar_l();
#pragma unroll
    for (int i = 0; i < 6; ++i) a[i] = *(LAS u32x2*)(KI + (i < 4 ? ka + (i >> 1) * 256 : kb) + (i & 1) * 512);
  }
#pragma unroll 1
  for (int G = 0; G < (h < 16 ? 3 : 5); ++G, tq += 2560, oa += 512) {
    if (G == 4) {
      oa = OC_OFF + (5 * (h - 16) + hf) * 16384 + (24 + w) * 512 + c32 * 16;
#pragma unroll 1
      for (unsigned spins = 0; spins < (1u << 16); ++spins) {
        bool ok = true;
#pragma unroll
        for (int i = 0; i < 6; ++i) {
          a[i] = __builtin_amdgcn_raw_buffer_load_b64(rw, KV_OFF + (h - 16) * 3072 + (i < 4 ? ka + (i >> 1) * 256 : kb) + (i & 1) * 512, 0, SC1);
          ok &= a[i][1] == tag;
        }
        asm volatile("" ::: "memory");
        if (__all(ok)) break;
        __builtin_amdgcn_s_sleep(8);
      }
    }
    const bf16x8 A0 = __builtin_bit_cast(bf16x8, u32x4{a[0][0], a[1][0], a[2][0], a[3][0]}), A1 = __builtin_bit_cast(bf16x8, u32x4{hf ? 0u : a[4][0], hf ? 0u : a[5][0], 0u, 0u});
    unsigned o[2][6];
#pragma unroll
    for (int u = 0; u < 2; ++u) {
      const s16x4 b0 = trd(tq + hf * 128 + 8 * u), b1 = trd(tq + hf * 128 + 8 * u + 64), b2 = trd(tq + 8 * u + 256);
      f32x16 d;
#pragma unroll
      for (int r = 0; r < 16; ++r) d[r] = 0.f;
      d = __builtin_amdgcn_mfma_f32_32x32x16_bf16(A0, __builtin_bit_cast(bf16x8, __builtin_shufflevector(b0, b1, 0, 1, 2, 3, 4, 5, 6, 7)), d, 0, 0, 0);
      d = __builtin_amdgcn_mfma_f32_32x32x16_bf16(A1, __builtin_bit_cast(bf16x8, __builtin_shufflevector(b2, b2, 0, 1, 2, 3, 4, 5, 6, 7)), d, 0, 0, 0);
      const float inv = __builtin_amdgcn_rcpf((hf ? d[8] : d[12]) + 1e-6f);
#pragma unroll
      for (int e = 0; e < 6; ++e) o[u][e] = pk2(d[2 * e] * inv, d[2 * e + 1] * inv);
    }
    __builtin_amdgcn_raw_buffer_store_b128(u32x4{o[0][0], o[0][1], o[1][0], o[1][1]}, rw, oa, 0, SC1);
    __builtin_amdgcn_raw_buffer_store_b128(u32x4{o[0][2], o[0][3], o[1][2], o[1][3]}, rw, oa + 2 * 16384, 0, SC1);
    __builtin_amdgcn_raw_buffer_store_b128(u32x4{o[0][4], o[0][5], o[1][4], o[1][5]}, rw, hf ? OOB : oa + 4 * 16384, 0, SC1);
  }
  bar_l();
  return fo;
}

DI unsigned mktag(unsigned ep) { const unsigned t = ep * 2654435761u + 0x9E3779B9u; return t + (t == ep); }

__global__ __launch_bounds__(512, 2) __attribute__((amdgpu_waves_per_eu(2, 2))) void fused(Params p) {
  typedef const __attribute__((address_space(4))) rsrc_t* crs;
  const crs ka = (crs)__builtin_amdgcn_kernarg_segment_ptr();
  Rs R = {ka[0], ka[1], ka[2], ka[3], ka[4], ka[5], ka[6], ka[7], ka[8]};
  typedef const __attribute__((address_space(4))) unsigned* cu32;
  unsigned long long pc;
  asm volatile("s_getpc_b64 %0" : "=s"(pc));
  constexpr unsigned CODE_BYTES = 31360;
#define TAG_LOAD() ep = *(cu32)p.ws.p; __builtin_amdgcn_sched_barrier(0)
#define TAG_READY() asm volatile("" : "+s"(ep)::"memory"); tag = mktag(ep)
  __builtin_amdgcn_sched_barrier(0);
  const int w = __builtin_amdgcn_readfirstlane(threadIdx.x >> 6), l = threadIdx.x & 63;
  const int xcd = blockIdx.x & 7, idx = blockIdx.x >> 3;
  asm volatile("s_mov_b32 m0, %2\n\ts_nop 0\n\tglobal_load_lds_dword %0, %1"
               ::"v"(min((((blockIdx.x >> 3) & 7) * 32 + (threadIdx.x >> 4)) * 128u, CODE_BYTES - 128)), "s"(pc & ~127ull), "s"(SB) : "memory");
  const int rg = xcd >> 1, par = xcd & 1;
  const bool doq = idx < 30, dou = idx < (xcd < 6 ? 28 : 24), doy = idx < 20, doh = !doy & doq, doa = idx >= 28;
  const int tq = par * 30 + idx, rp = rg * 4 + (tq & 3), ct = tq >> 2;
  const int ft = rg * 7 + (idx >> 2), dp = par * 4 + (idx & 3);
  const int yst = rg * 4 + (idx & 3), ydt = par * 5 + (idx >> 2);
  const int csa = par * 8 + rg * 2 + (idx - 30);
  const int j = w & 3;
  unsigned ep, tag, po;
  unsigned tlq = 0, tlu = 0, tly = 0, tlo = 0;
  unsigned b2[2];
#define LN_LOAD() \
    const unsigned xo = (8 * LNC + 2 * w + (l >> 5)) * 1280; \
    u32x2 u[5]; \
_Pragma("unroll") \
    for (int i = 0; i < 5; ++i) u[i] = __builtin_amdgcn_raw_buffer_load_b64(R.x, xo + (l & 31) * 8 + i * 256, 0, 0); \
    const unsigned first = __builtin_amdgcn_raw_buffer_load_b32(R.x, xo, 0, 0);
#define LN_PRE()                                                                                                                         \
  unsigned ta = LNB + ((l & 15) >> 2) * LNRS + (l >> 4) * 32 + (l & 3) * 8 + j * 128, ho[3];                                              \
  _Pragma("unroll") for (int a = 0; a < 3; ++a) { const int d = 64 * (j + 4 * a) + l; ho[a] = HT_OFF + (d / 80) * 327680 + (d % 80) * 16 + LNC * 1280; } \
  if (j >= 2) ho[2] = l < 4 ? ST_OFF + LNC * 64 + l * 16 : OOB;                                                                          \
  PIN(ta); PIN(ho[0]); PIN(ho[1]); PIN(ho[2])
  if (__builtin_expect(!doq, 0)) {
    u32x4 cv[20];
    const unsigned wo0 = csa < 15 ? (4 * j + (l >> 4)) * 3840 + csa * 256 + (l & 15) * 16 : OOB;
    if (w < 4) {
      LN_LOAD()
      TAG_LOAD();
      ln_rows(u, first, LNB + (2 * w + (l >> 5)) * LNRS + (l & 31) * 8, LNB + 8 * LNRS + (l & 1) * 32 + (2 * w + (l >> 5)) * 4, l);
      TAG_READY();
      bar_l();
      bar();
#pragma unroll
      for (int i = 0; i < 20; ++i) cv[i] = __builtin_amdgcn_raw_buffer_load_b128(R.wq, wo0, (20 + i) * 16 * 3840, 0);
    } else {
#pragma unroll
      for (int i = 0; i < 12; ++i) cv[i] = __builtin_amdgcn_raw_buffer_load_b128(R.wq, wo0, i * 16 * 3840, 0);
      TAG_LOAD();
      LN_PRE();
      TAG_READY();
      bar();
      ln_store(R.ws, ta, ho, j, l);
      asm volatile("" ::: "memory");
      SCHED();
#pragma unroll
      for (int i = 12; i < 20; ++i) cv[i] = __builtin_amdgcn_raw_buffer_load_b128(R.wq, wo0, i * 16 * 3840, 0);
      bar_v<8>();
      if (j == 0) flag(R.ws, F1_OFF + blockIdx.x * 64, tag, l);
    }
    f32x2 c[4] = {{0, 0}, {0, 0}, {0, 0}, {0, 0}};
#pragma unroll
    for (int i = 0; i < 20; ++i)
#pragma unroll
      for (int e = 0; e < 4; ++e) c[e] += f32x2{bflo(cv[i][e]), bfhi(cv[i][e])};
    *(LAS f32x4*)(65536 + threadIdx.x * 32) = f32x4{c[0][0], c[0][1], c[1][0], c[1][1]};
    *(LAS f32x4*)(65536 + threadIdx.x * 32 + 16) = f32x4{c[2][0], c[2][1], c[3][0], c[3][1]};
    bar_l();
    float s0 = 0.f;
#pragma unroll
    for (int i = 0; i < 8; ++i) s0 += *(LAS float*)(65536 + ((l >> 4) * 8 + i) * 512 + (w * 16 + (l & 15)) * 4);
    const auto s1 = __builtin_amdgcn_permlane16_swap(__builtin_bit_cast(unsigned, s0), __builtin_bit_cast(unsigned, s0), false, false);
    s0 = __builtin_bit_cast(float, (unsigned)s1[0]) + __builtin_bit_cast(float, (unsigned)s1[1]);
    const auto s2 = __builtin_amdgcn_permlane32_swap(__builtin_bit_cast(unsigned, s0), __builtin_bit_cast(unsigned, s0), false, false);
    s0 = __builtin_bit_cast(float, (unsigned)s2[0]) + __builtin_bit_cast(float, (unsigned)s2[1]);
    __builtin_amdgcn_raw_buffer_store_b64(u32x2{__builtin_bit_cast(unsigned, s0), tag}, R.ws, l < 16 ? CS_OFF + (csa * 128 + w * 16 + l) * 8 : OOB, 0, SC1);
    bar();
  } else if (w < 4) {
    LN_LOAD()
    TAG_LOAD();
    u32x2 bb = __builtin_amdgcn_raw_buffer_load_b64(R.b1, (ft * 64 + 32 * (w & 1) + 16 * (w >> 1) + 4 * (l >> 4)) * 2, 0, 0);
#pragma unroll
    for (int n = 0; n < 2; ++n) b2[n] = __builtin_amdgcn_raw_buffer_load_b16(R.b2, (yst * 128 + 32 * w + 16 * n + (l & 15)) * 2, 0, 0);
    UPre up;
    u_pre(up, ft, dp, F2_OFF + (ct * 16 + rp) * 64, w, l);
    QPre qp;
    q_pre(qp, rp, ct, w, l);
    unsigned lo = LNB + (2 * w + (l >> 5)) * LNRS + (l & 31) * 8, so = LNB + 8 * LNRS + (l & 1) * 32 + (2 * w + (l >> 5)) * 4;
    PIN(lo); PIN(so);
    f32x4 acc[4][4];
#pragma unroll
    for (int m = 0; m < 4; ++m)
#pragma unroll
      for (int n = 0; n < 4; ++n) { acc[m][n] = f32x4{0, 0, 0, 0}; PIN(acc[m][n]); }
    SCHED();
    ln_rows(u, first, lo, so, l);
    TAG_READY();
    PIN(bb); PIN(b2[0]); PIN(b2[1]);
    bar_l();
    q_compute(p, R.ws, qp, acc, ct, tag, w, l);
    if (dou) {
      u_compute(p, R.ws, up, bb, tag, w, l);
    } else {
      bar_v<0>();
      __builtin_amdgcn_raw_buffer_store_b32(tag, R.ws, up.fo, 0, SC1);
    }
  } else {
    {
      unsigned vx[5], vw[4];
      vx[0] = (rp * 128 + 32 * j + (l >> 3)) * 1280 + (((l & 7) ^ ((l >> 4) & 1)) * 16);
#pragma unroll
      for (int i = 1; i < 4; ++i) vx[i] = (vx[0] ^ (32 * i)) + i * (8 * 1280 - 1024);
      off256(vw, 3840, ct * 256, j, l);
      const int cq = (rp + ct) & 3;
      const unsigned long long qord = cq & 2 ? (cq & 1 ? QORD3 : QORD2) : (cq & 1 ? QORD1 : QORD0);
      const int k0 = qord & 15;
      issue<4>(vw, R.wq, k0 * (64 * 3840), QB + 65536 + j * 4096);
      issue<4>(vx, R.x, k0 * 128, QB + 65536 + 16384 + j * 4096);
      const int kk = (qord >> (4 * (l & 15))) & 15;
      unsigned tqa = kk * 128, tqb = kk * (64 * 3840), tql = QB + ((l + 2) & 3) * 32768 + j * 4096;
      unsigned f1o = F1_OFF + (64 * j + l) * 64, sto = ((j & 1) ? CS_OFF + ct * 1024 : ST_OFF + rp * 1024) + l * 16;
      PIN(f1o); PIN(sto);
      LN_PRE();
      ULd ul;
      u_ld(ul, ft, dp, j, l);
      stream<4, 16, 1>(vx, vw, R.x, R.wq, tqa, tqb, tql, 16384, 1, 3, 3, tlq, R.ws, ep);
      tag = mktag(ep);
      bar_v<16>();
#define QS0 3
      ln_store(R.ws, ta, ho, j, l);
      stream<4, 16>(vx, vw, R.x, R.wq, tqa, tqb, tql, 16384, QS0, 10, 4, tlq, R.ws, po);
      unsigned fv;
      if (dou) {
#define U_IN [a0] "v"(ul.a[0]), [a4] "v"(ul.a[4]), [b0] "v"(ul.b[0]), [b1] "v"(ul.b[1]), [b2] "v"(ul.b[2]), [b3] "v"(ul.b[3]), [cu] "s"((dp + ft) & 3), \
             [ws] "s"(R.ws), [w1] "s"(R.w1), [j9] "s"(j * 9216), [sp] "s"((8 * j + dp) * 3456 + ft * 128)
        asm volatile(U1_ASM : [fv] "=&v"(fv) : U_IN, [f1] "v"(f1o), [st] "v"(sto), [sb] "s"(SB + (j & 1) * 1024), [tag] "s"(tag) : "memory", "scc", "vcc", S_CLOB);
        SCHED();
        constexpr int NY = NT - 10;
        unsigned yb[4], zb[4];
        off256(yb, 4096, yst * 256, j, l);
        unsigned yx0 = (yst * 128 + 32 * j + (l >> 3)) * 1280 + ydt * 128 + (((l & 7) ^ (l >> 4)) * 16), yx1 = (yx0 ^ 64) + 8 * 1280 - 1024;
        unsigned za0 = (16 * j + (l >> 3)) * 1280 + ydt * 128 + (((l & 7) ^ ((l >> 3) & 2)) * 16), za1 = (za0 ^ 64) + 8 * 1280 - 1024;
        zb[0] = OC_OFF + 4 * j * 16384 + yst * 1024 + l * 16;
#pragma unroll
        for (int i = 1; i < 4; ++i) zb[i] = (zb[0] ^ ((i >> 1) * 128)) + i * (16384 - 1024);
        const int p0 = (64 * ydt) / 80, p1 = (64 * ydt + 63) / 80;
        unsigned ya = GP_OFF + ydt * 221184 + j * 2048 + l * 16, yo = AO_OFF + (8 * ydt + 2 * j) * 32768 + yst * 2048 + l * 16;
        unsigned f3o = F3_OFF + ((l < 32 ? p0 : p1) * 32 + NH + min(l & 31, NY - 1)) * 64, f4o = F4_OFF + (yst * 64 + l) * 64, f5o = F5_OFF + (yst * 5 + (ydt >> 1)) * 64;
        PIN(ya); PIN(yo); PIN(za0); PIN(za1); PIN(yx0); PIN(yx1); PIN(f3o); PIN(f4o); PIN(f5o);
#pragma unroll
        for (int i = 0; i < 4; ++i) { PIN(yb[i]); PIN(zb[i]); }
        asm volatile(UL_ASM ::U_IN : "memory", "scc", S_CLOB);
        if (doy) {
          asm volatile(YL_ASM
                       : [fv] "=&v"(fv)
                       : [yb0] "v"(yb[0]), [yb1] "v"(yb[1]), [yb2] "v"(yb[2]), [yb3] "v"(yb[3]), [zb0] "v"(zb[0]), [zb1] "v"(zb[1]), [zb2] "v"(zb[2]),
                         [zb3] "v"(zb[3]), [ya] "v"(ya), [za0] "v"(za0), [za1] "v"(za1), [yx0] "v"(yx0), [yx1] "v"(yx1), [yo] "v"(yo), [f3] "v"(f3o),
                         [f4] "v"(f4o), [f5] "v"(f5o), [cy] "s"((yst + ydt) & 3), [ws] "s"(R.ws), [w2] "s"(R.w2), [wo] "s"(R.wo), [x] "s"(R.x),
                         [tag] "s"(tag), [j2] "s"(j * 2048)
                       : "memory", "scc", "vcc", S_CLOB);
          if (blockIdx.x == 0 && w == 4) flag(R.ws, EP_OFF, tag, l);
        } else {
          asm volatile(TL_ASM ::: "memory");
        }
      } else {
        bar_v<16>(); bar_v<8>();
        poll1<1>(R.ws, f1o, tag);
        bar();
        if (j < 2) dma_sc1(sto, R.ws, 0, SB + j * 1024);
        bar_v<0>();
        bar();
      }
    }
  }
  if (!dou) {
#define WARM(o, r) asm volatile("s_mov_b32 m0, %2\n\ts_nop 0\n\tbuffer_load_dword %0, %1, 0 offen lds" ::"v"(o), "s"(r), "s"(SB) : "memory")
    if (doa) {
      const int i0 = idx < 30 ? 16 * PFB + (idx - 28) * 8 * PFA : (idx - 30) * 8 * PFB, n = idx < 30 ? PFA : PFB;
      unsigned o = (2 * ((i0 + w) * 8 + (l >> 3)) + par) * 4096 + rg * 1024 + (l & 7) * 128;
#pragma unroll 1
      for (int i = 0; i < n; ++i, o += 128 * 4096) WARM(o, R.w2);
    } else {
      unsigned o = 16 * (16 * PFB + 16 * PFA) * 4096 + (((par * 4 + idx - 24) * 8 + w) * 64 + l) * 128;
#pragma unroll 1
      for (int i = 0; i < PFC; ++i, o += 128 * 4096) WARM(o, R.w2);
      o = (((par * 4 + idx - 24) * 8 + w) * 100 + l) * 128;
      WARM(o, R.wo); WARM(o + 36 * 128, R.wo);
    }
    wait_v<0>();
  }
  unsigned f4w = OOB;
  if (doa) {
    f4w = attn(p, R.ws, (idx - 28) * 8 + xcd, tag, w, l);
    if (w < 4 || !doh) { wait_v<0>(); __builtin_amdgcn_raw_buffer_store_b32(tag, R.ws, f4w, 0, SC1); }
  }
  const int hst = xcd * 2 + (idx & 1), hdt = (idx - 20) >> 1;
  if (w < 4) {
    if (doy) y_compute(p, R.out, b2, yst, ydt, j, l);
    if (doh) o_compute(p, R.ws, hst, hdt, tag, j, l);
  } else if (doh) {
    unsigned va[5], vb[4];
    off256(vb, 4096, hst * 256, j, l);
    va[0] = GP_OFF + (2 * hdt + (j >> 1)) * 221184 + (j & 1) * 4096 + l * 16;
    const unsigned fo = F3_OFF + (min((128 * hdt) / 80 + (l >> 4), (128 * hdt + 127) / 80) * 32 + min(l & 15, NH - 1)) * 64;
    unsigned fv;
    asm volatile("buffer_load_dword %0, %1, %2, 0 offen sc1" : "=&v"(fv) : "v"(fo), "s"(R.ws) : "memory");
#define HB(k) issue<4>(vb, R.w2, (k) * (64 * 4096), (k) * 32768 + j * 4096)
#define HA(k) issue<4>(va, R.ws, (k) * 8192, (k) * 32768 + 16384 + j * 4096)
    HB(0);
    unsigned ta = l * 8192, tb = l * (64 * 4096), tl = (l & 3) * 32768 + j * 4096;
    PIN(ta); PIN(tb); PIN(tl); PIN(va[0]);
    va[1] = va[2] = va[3] = va[0];
    asm volatile("s_waitcnt vmcnt(4)" : "+v"(fv)::"memory");
    if (doa) __builtin_amdgcn_raw_buffer_store_b32(tag, R.ws, f4w, 0, SC1);
    if (__all(fv == tag)) {
      HA(0); HB(1); HA(1);
      bar_v<8>();
      HB(2); HA(2);
    } else {
      HB(1); HB(2);
      poll1<PSL>(R.ws, fo, tag);
      HA(0); HA(1); HA(2);
      bar_v<8>();
    }
    stream<4, 12>(va, vb, R.ws, R.w2, ta, tb, tl, 16384, 3, NH, 4, tlo, R.ws, po);
    bar_v<16>(); bar_v<8>(); bar_v<0>(); bar();
    bar();
  }
}

extern "C" void kernel_launch(void* const* d_in, const int* in_sizes, int n_in, void* d_out, int out_size, void* d_ws, size_t ws_size,
                              hipStream_t stream) {
  Params p;
  const unsigned C = 0x00020000;
  p.x = {d_in[0], S * D * 2, C}; p.wqkv = {d_in[1], D * N3 * 2, C}; p.ws = {d_ws, WS_END, C}; p.w1 = {d_in[4], S * F * 2, C};
  p.w2 = {d_in[6], S * F * 2, C}; p.wo = {d_in[2], D * D * 2, C}; p.b1 = {d_in[5], F * 2, C}; p.b2 = {d_in[7], S * 2, C};
  p.out = {d_out, S * D * 2, C};
  (void)hipFuncSetAttribute((const void*)fused, hipFuncAttributeMaxDynamicSharedMemorySize, LDS_BYTES);
  fused<<<256, 512, LDS_BYTES, stream>>>(p);
}
```
